```python
import torch

def _softmax(x, axis=-1):
    return torch.softmax(x, dim=axis)
def _gelu(x):
    return torch.nn.functional.gelu(x, approximate="tanh")
def _silu(x):
    return torch.nn.functional.silu(x)
def _sigmoid(x):
    return torch.sigmoid(x)
def _layernorm(x, axis=-1, eps=1e-5):
    m = x.mean(dim=axis, keepdim=True)
    v = x.var(dim=axis, keepdim=True, unbiased=False)
    return (x - m) / torch.sqrt(v + eps)
def _scatter_add(tgt, idx, src, axis=0):
    return tgt.clone().index_add_(axis, idx, src)
def _segment_sum(src, seg, n):
    out = torch.zeros((n,) + src.shape[1:], dtype=src.dtype)
    return out.index_add_(0, seg, src)
def _pad(x, pw):
    flat = tuple(v for pair in reversed(pw) for v in pair)
    return torch.nn.functional.pad(x, flat)

kernel_name = 'parallel_sum__sliding_local_dilated__mixer_token_mlp__prompt'

def setup_inputs():

    _w = (lambda a: (lambda t: t.to(torch.bfloat16) if t.is_floating_point() else t)(torch.as_tensor(a)))
    def _A_setup():
        B, S, D = 1, 2048, 512
        return {
            "x": _w((lambda *s: torch.randn(*s, dtype=torch.bfloat16) * 0.05)(B, S, D)),
            "wqkv": _w((lambda *s: torch.randn(*s, dtype=torch.bfloat16) * 0.05)(D, 3 * D)),
            "wo": _w((lambda *s: torch.randn(*s, dtype=torch.bfloat16) * 0.05)(D, D)),
        }
    _A_in = _A_setup()

    def _B_setup():
        B, S, D, F = 1, 2048, 512, 1376
        return {
            "x": _w((lambda *s: torch.randn(*s, dtype=torch.bfloat16) * 0.05)(B, S, D)),
            "w1": _w((lambda *s: torch.randn(*s, dtype=torch.bfloat16) * 1.6)(S, F) * (S ** -0.5)),
            "b1": _w((lambda *s: torch.randn(*s, dtype=torch.bfloat16) * 12.8)(F) * 0.02),
            "w2": _w((lambda *s: torch.randn(*s, dtype=torch.bfloat16) * 0.8)(F, S) * (F ** -0.5)),
            "b2": _w((lambda *s: torch.randn(*s, dtype=torch.bfloat16) * 12.8)(S) * 0.02),
        }
    _B_in = _B_setup()

    return {
        "A_x": _A_in["x"],
        "A_wqkv": _A_in["wqkv"],
        "A_wo": _A_in["wo"],
        "B_x": _B_in["x"],
        "B_w1": _B_in["w1"],
        "B_b1": _B_in["b1"],
        "B_w2": _B_in["w2"],
        "B_b2": _B_in["b2"],
    }

def reference(A_x, A_wqkv, A_wo, B_x, B_w1, B_b1, B_w2, B_b2):
    def _A_ref(x, wqkv, wo):
        B, S, D = x.shape
        H = 32; Dh = D // H
        W, dil = 8, 2
        qkv = (lambda x, *d: x.permute(*d))((x @ wqkv).reshape(B, S, 3, H, Dh), 2, 0, 3, 1, 4)
        q, k, v = qkv[0], qkv[1], qkv[2]
        scale = Dh ** -0.5
        pos = torch.arange(S)
        off = torch.arange(W) * dil
        idx = pos[:, None] - off[None, :]
        mask = idx >= 0
        idx_c = torch.where(mask, idx, 0).reshape(-1)
        kg = k[:, :, idx_c, :].reshape(B, H, S, W, Dh)
        vg = v[:, :, idx_c, :].reshape(B, H, S, W, Dh)
        logits = torch.einsum("bhsd,bhswd->bhsw", q, kg) * scale
        logits = torch.where(mask[None, None, :, :], logits, -1e9)
        attn = _softmax(logits, axis=-1)
        o = torch.einsum("bhsw,bhswd->bhsd", attn, vg)
        return (lambda x, *d: x.permute(*d))(o, 0, 2, 1, 3).reshape(B, S, D) @ wo

    def _B_ref(x, w1, b1, w2, b2):
        B, S, D = x.shape
        h = _layernorm(x, axis=-1)
        u = torch.einsum("bsd,sf->bdf", h, w1) + b1
        u = _gelu(u)
        y = torch.einsum("bdf,fs->bds", u, w2) + b2
        return (lambda x, *d: x.permute(*d))(y, 0, 2, 1)

    x = A_x
    xn = _layernorm(x, axis=-1)
    return x + _A_ref(xn, A_wqkv, A_wo) + _B_ref(xn, B_w1, B_b1, B_w2, B_b2)
```

```cpp
#include <hip/hip_runtime.h>
#include <hip/hip_bf16.h>
typedef unsigned short u16;
__device__ __forceinline__ float bf2f(u16 b){ return __builtin_bit_cast(float,(unsigned)b<<16); }
__device__ __forceinline__ u16 f2bf(float f){ __hip_bfloat16 h=__float2bfloat16(f); return __builtin_bit_cast(u16,h); }
__device__ __forceinline__ float rbf(float f){ return bf2f(f2bf(f)); }
constexpr int S=2048,D=512,F=1376,H=32,DH=16;

__global__ void k_ln(const u16* x, u16* xn, u16* h){
  int row=blockIdx.x; int t=threadIdx.x;
  float v[8]; float s=0;
  for(int j=0;j<8;j++){ v[j]=bf2f(x[row*D+t*8+j]); s+=v[j]; }
  for(int o=32;o;o>>=1) s+=__shfl_xor(s,o);
  float m=s/D; float q=0;
  for(int j=0;j<8;j++){ float d=v[j]-m; q+=d*d; }
  for(int o=32;o;o>>=1) q+=__shfl_xor(q,o);
  float r=rsqrtf(q/D+1e-5f);
  s=0;
  for(int j=0;j<8;j++){ v[j]=rbf((v[j]-m)*r); xn[row*D+t*8+j]=f2bf(v[j]); s+=v[j]; }
  for(int o=32;o;o>>=1) s+=__shfl_xor(s,o);
  m=s/D; q=0;
  for(int j=0;j<8;j++){ float d=v[j]-m; q+=d*d; }
  for(int o=32;o;o>>=1) q+=__shfl_xor(q,o);
  r=rsqrtf(q/D+1e-5f);
  for(int j=0;j<8;j++){ h[row*D+t*8+j]=f2bf((v[j]-m)*r); }
}
__global__ void k_qkv(const u16* xn,const u16* w,u16* qkv){
  int n=blockIdx.x*256+threadIdx.x; int s=blockIdx.y;
  float a=0; for(int k=0;k<D;k++) a+=bf2f(xn[s*D+k])*bf2f(w[k*1536+n]);
  qkv[s*1536+n]=f2bf(a);
}
__global__ void k_attn(const u16* qkv,u16* o){
  int s=blockIdx.x; int hh=threadIdx.x; if(hh>=H) return;
  float lg[8]; float mx=-1e30f;
  for(int w=0;w<8;w++){ int p=s-2*w; float a=0; if(p>=0){ for(int d=0;d<DH;d++) a+=bf2f(qkv[s*1536+hh*16+d])*bf2f(qkv[p*1536+512+hh*16+d]); a*=0.25f;} else a=-1e9f; lg[w]=a; mx=fmaxf(mx,a);}
  float sum=0; for(int w=0;w<8;w++){ lg[w]=__expf(lg[w]-mx); sum+=lg[w]; }
  for(int d=0;d<DH;d++){ float a=0; for(int w=0;w<8;w++){ int p=s-2*w; if(p<0)p=0; a+=lg[w]*bf2f(qkv[p*1536+1024+hh*16+d]); } o[s*D+hh*16+d]=f2bf(a/sum); }
}
__global__ void k_w1(const u16* h,const u16* w1,const u16* b1,u16* u){
  int f=blockIdx.x*32+ (threadIdx.x&31); int d=blockIdx.y*8+(threadIdx.x>>5);
  float a=0; for(int s=0;s<S;s++) a+=bf2f(h[s*D+d])*bf2f(w1[s*F+f]);
  a+=bf2f(b1[f]);
  float t=0.7978845608f*(a+0.044715f*a*a*a);
  float g=0.5f*a*(1.f+tanhf(t));
  u[d*F+f]=f2bf(g);
}
__global__ void k_fin(const u16* x,const u16* o,const u16* wo,const u16* u,const u16* w2,const u16* b2,u16* out){
  int d=blockIdx.x*256+threadIdx.x; int s=blockIdx.y;
  float a=0; for(int k=0;k<D;k++) a+=bf2f(o[s*D+k])*bf2f(wo[k*D+d]);
  float b=0; for(int f=0;f<F;f++) b+=bf2f(u[d*F+f])*bf2f(w2[f*S+s]);
  out[s*D+d]=f2bf(bf2f(x[s*D+d])+a+b+bf2f(b2[s]));
}
extern "C" void kernel_launch(void* const* d_in, const int* in_sizes, int n_in, void* d_out, int out_size, void* d_ws, size_t ws_size, hipStream_t stream){
  const u16* x=(const u16*)d_in[0]; const u16* wqkv=(const u16*)d_in[1]; const u16* wo=(const u16*)d_in[2];
  const u16* w1=(const u16*)d_in[4]; const u16* b1=(const u16*)d_in[5]; const u16* w2=(const u16*)d_in[6]; const u16* b2=(const u16*)d_in[7];
  u16* ws=(u16*)d_ws; u16* xn=ws; u16* h=xn+S*D; u16* qkv=h+S*D; u16* o=qkv+S*1536; u16* u=o+S*D;
  k_ln<<<S,64,0,stream>>>(x,xn,h);
  k_qkv<<<dim3(6,S),256,0,stream>>>(xn,wqkv,qkv);
  k_attn<<<S,64,0,stream>>>(qkv,o);
  k_w1<<<dim3(43,64),256,0,stream>>>(h,w1,b1,u);
  k_fin<<<dim3(2,S),256,0,stream>>>(x,o,wo,u,w2,b2,(u16*)d_out);
}
```

```cpp
#include <hip/hip_runtime.h>
#pragma clang diagnostic ignored "-Wint-to-pointer-cast"

typedef unsigned u32;
typedef unsigned long long u64;
typedef u32 v4u __attribute__((ext_vector_type(4)));
typedef u32 v2u __attribute__((ext_vector_type(2)));
typedef float f4 __attribute__((ext_vector_type(4)));
typedef float f2 __attribute__((ext_vector_type(2)));
typedef short s4 __attribute__((ext_vector_type(4)));
typedef short s8 __attribute__((ext_vector_type(8)));
typedef __bf16 bf8 __attribute__((ext_vector_type(8)));
typedef __bf16 bf2 __attribute__((ext_vector_type(2)));
typedef __amdgpu_buffer_rsrc_t rsrc_t;
#define LAS __attribute__((address_space(3)))
#define DI __device__ __forceinline__

constexpr int S = 2048, D = 512, F = 1376;
constexpr u32 WS_E = 0, WS_AFLAG = 4096, WS_HFLAG = 8192, WS_OFLAG = 12288, WS_RFLAG = 20480, WS_UFLAG = 65536,
              WS_STATS = 81920, WS_H = 1u << 20, WS_O = 3u << 20, WS_U = 5u << 20, WS_R = 7u << 20, WS_END = 11u << 20;
constexpr u32 WS_STAMP = 16u << 20;
constexpr u32 OOB = 0x80000000u;
constexpr int SC1 = 16;
constexpr u32 LDS_BYTES = 163840;
constexpr u32 L_MISC = 39 * 4096;
constexpr u32 L_LN = 131072;
constexpr u32 L_LNST = L_LN + 16 * 1088;
constexpr u32 Q_STAGE = 45056, Q_X = 24576, L_QDUMMY = 3 * Q_STAGE;
constexpr u32 L_QST2 = L_QDUMMY + 1024;
constexpr u32 L_OST = L_QST2 + 8 * 640;
static_assert(L_OST + 18 * 1024 <= L_MISC, "map");
constexpr u32 SPIN_CAP = 1u << 18;

#define STAMPS 0
#define STAMP(i) do { } while (0)

DI rsrc_t mk(const void* p, u32 n) { return __builtin_amdgcn_make_buffer_rsrc((void*)p, (short)0, (int)n, 0x00020000); }
DI void glds(rsrc_t rs, u32 voff, u32 soff, u32 lds) {
  asm volatile("s_mov_b32 m0, %3\n\ts_nop 3\n\tbuffer_load_dwordx4 %0, %1, %2 offen lds" ::"v"(voff), "s"(rs), "s"(soff), "s"(lds) : "memory");
}
#define WAITV(n) asm volatile("s_waitcnt vmcnt(" #n ")" ::: "memory")
#define BAR() asm volatile("s_waitcnt lgkmcnt(0)\n\ts_barrier" ::: "memory")
#define FENCE() asm volatile("" ::: "memory")
#define SCHED() __builtin_amdgcn_sched_barrier(0)
DI v4u ld128(rsrc_t rs, u32 voff, u32 soff) { return __builtin_amdgcn_raw_buffer_load_b128(rs, voff, soff, SC1); }
DI v2u ld64(rsrc_t rs, u32 voff, u32 soff) { return __builtin_amdgcn_raw_buffer_load_b64(rs, voff, soff, SC1); }
DI void st128(v4u v, rsrc_t rs, u32 voff, u32 soff) { __builtin_amdgcn_raw_buffer_store_b128(v, rs, voff + soff, 0, SC1); }
DI void st64(v2u v, rsrc_t rs, u32 voff, u32 soff) { __builtin_amdgcn_raw_buffer_store_b64(v, rs, voff, soff, SC1); }
DI float asf(u32 x) { return __builtin_bit_cast(float, x); }
DI u32 asu(float x) { return __builtin_bit_cast(u32, x); }
DI float bflo(u32 x) { return asf(x << 16); }
DI float bfhi(u32 x) { return asf(x & 0xffff0000u); }
DI u32 pk(float a, float b) { f2 p = {a, b}; bf2 q = __builtin_convertvector(p, bf2); return __builtin_bit_cast(u32, q); }
DI f4 mma(v4u a, v4u b, f4 c) {
  return __builtin_amdgcn_mfma_f32_16x16x32_bf16(__builtin_bit_cast(bf8, a), __builtin_bit_cast(bf8, b), c, 0, 0, 0);
}
DI f4 mma16(v2u a, v2u b, f4 c) {
  return __builtin_amdgcn_mfma_f32_16x16x16bf16_1k(__builtin_bit_cast(s4, a), __builtin_bit_cast(s4, b), c, 0, 0, 0);
}
DI v2u trd(u32 a) { return __builtin_bit_cast(v2u, __builtin_amdgcn_ds_read_tr16_b64_v4i16((LAS s4*)a)); }
DI v4u wfrag(u32 a) { v2u lo = trd(a), hi = trd(a + 2048); return v4u{lo.x, lo.y, hi.x, hi.y}; }
DI v4u lds128(u32 a) { return *(LAS v4u*)a; }
template <int CTRL> DI float dpp(float v) { return asf((u32)__builtin_amdgcn_update_dpp(0, (int)asu(v), CTRL, 0xf, 0xf, true)); }
DI float x16(float v, float& o) { u32 y = asu(v); auto r = __builtin_amdgcn_permlane16_swap(y, y, false, false); o = asf((u32)r[1]); return asf((u32)r[0]); }
DI float x32(float v, float& o) { u32 y = asu(v); auto r = __builtin_amdgcn_permlane32_swap(y, y, false, false); o = asf((u32)r[1]); return asf((u32)r[0]); }
DI float sum_groups(float v) { float o, a = x32(v, o); v = a + o; a = x16(v, o); return a + o; }
DI float max_groups(float v) { float o, a = x32(v, o); v = fmaxf(a, o); a = x16(v, o); return fmaxf(a, o); }
DI float wave_sum(float v) {
  v += dpp<0xB1>(v);
  v += dpp<0x4E>(v);
  v += dpp<0x141>(v);
  v += dpp<0x140>(v);
  return sum_groups(v);
}
DI void poll64(rsrc_t ws, u32 voff, u32 soff, bool act, u32 tl, u32 th) {
#pragma unroll 1
  for (u32 n = 0; n < SPIN_CAP; ++n) {
    v2u f = ld64(ws, voff, soff);
    if (__all(!act || (f.x == tl && f.y == th))) break;
    asm volatile("s_sleep 2" ::: "memory");
  }
  FENCE();
}
DI void poll256(rsrc_t ws, u32 voff, u32 soff, u32 tl, u32 th) {
#pragma unroll 1
  for (u32 n = 0; n < SPIN_CAP; ++n) {
    v4u f = ld128(ws, voff, soff), g = ld128(ws, voff + 1024, soff);
    bool ok = f.x == tl && f.y == th && f.z == tl && f.w == th && g.x == tl && g.y == th && g.z == tl && g.w == th;
    if (__all(ok)) break;
    asm volatile("s_sleep 2" ::: "memory");
  }
  FENCE();
}
DI void post(rsrc_t ws, u32 off, int lane, u32 tl, u32 th) {
  if (lane == 0) st64(v2u{tl, th}, ws, off, 0);
}
DI void hbar(u32 target, int lane) {
  asm volatile("s_waitcnt lgkmcnt(0)" ::: "memory");
  if (lane == 0) __hip_atomic_fetch_add((LAS u32*)L_MISC, 1u, __ATOMIC_RELAXED, __HIP_MEMORY_SCOPE_WORKGROUP);
#pragma unroll 1
  for (u32 n = 0; n < (1u << 24); ++n) {
    u32 v = __hip_atomic_load((LAS u32*)L_MISC, __ATOMIC_RELAXED, __HIP_MEMORY_SCOPE_WORKGROUP);
    if (__builtin_amdgcn_readfirstlane(v) >= target) break;
  }
  FENCE();
}

__global__ __launch_bounds__(512, 2) void fused_kernel(const void* __restrict__ px, const void* __restrict__ pwqkv, const void* __restrict__ pwo,
                                                     const void* __restrict__ pw1, const void* __restrict__ pb1, const void* __restrict__ pw2,
                                                     const void* __restrict__ pb2, void* __restrict__ pout, void* __restrict__ pws) {
  __shared__ __attribute__((aligned(1024))) char lds_mem[LDS_BYTES];
  asm volatile("" ::"v"((u32)(size_t)(LAS char*)lds_mem));
  STAMP(0);
  const int tid = threadIdx.x, lane = tid & 63;
  const int w = __builtin_amdgcn_readfirstlane(tid >> 6);
  const int b = blockIdx.x, xcd = b & 7, r = b >> 3;
  const int g = lane >> 4, i16 = lane & 15;
  const rsrc_t ws = mk(pws, WS_END), rx = mk(px, S * D * 2);

  asm volatile("" ::"s"(px), "s"(pwqkv), "s"(pwo), "s"(pw1), "s"(pb1), "s"(pw2), "s"(pb2), "s"(pout), "s"(pws));
  if (w == 6)
    asm volatile("s_mov_b32 m0, %2\n\ts_nop 3\n\tbuffer_load_dword %0, %1, 0 offen sc1 lds" ::"v"(lane < 2 ? (u32)(lane * 4) : OOB), "s"(ws), "s"(L_MISC + 512) : "memory");
  {
    u64 pc;
    u32 len;
    asm volatile("s_getpc_b64 %0\n.Lfk_pc:\n\ts_mov_b32 %1, .Lfk_end-.Lfk_pc" : "=s"(pc), "=s"(len));
    if (w == 7) {
      const rsrc_t rc = mk((const void*)pc, len);
      const u32 vo = lane < 12 ? (u32)((r * 12 + lane) * 128) : OOB;
      asm volatile("s_mov_b32 m0, %2\n\ts_nop 3\n\tbuffer_load_dword %0, %1, 0 offen lds" ::"v"(vo), "s"(rc), "s"(L_MISC + 1024) : "memory");
    }
  }
  if (tid == 0) *(LAS u32*)L_MISC = 0u;

  const u32 wbase = (u32)((4 * g + (i16 >> 2)) * 128 + (((2 * g + (i16 >> 3)) & 3) << 5) + 8 * (i16 & 3));
  const int qtr = w & 3;
  const int rho = 8 * qtr + (lane >> 3);
  const u32 csrc = (u32)(((lane & 7) ^ (((rho >> 1) & 3) << 1)) * 16);
  const int knat = ((rho >> 2) & 3) * 8 + (rho >> 4) * 4 + (rho & 3);

  const int ncol = (xcd & 1) ? 5 : 6, nU = 3 * ncol;
  const bool isU = r < nU;
  const int qi = ((xcd & 3) == 0 ? 0 : (xcd & 3) == 1 ? 14 : (xcd & 3) == 2 ? 30 : 44) + (r - nU);
  const bool isQ = !isU && (r - nU) < ((xcd & 1) ? 16 : 14);
  const int st = 8 * (xcd >> 1) + (r >> 2), T = 4 * (xcd & 1) + (r & 3);
  const rsrc_t rw2 = mk(pw2, F * S * 2), rwo = mk(pwo, D * D * 2);
  const int rotf = ((r & 3) * 11 + (r >> 2) * 5) % 43, rotc = (r >> 2) * 2;
  const u32 v_w2 = (u32)rho * 4096 + csrc, v_wo = (u32)rho * 1024 + csrc;
  auto f_issue = [&](int n) {
    if (n < 16) {
      int cs = (n + rotc) & 15;
      glds(rwo, v_wo, (u32)(cs * 32 * 1024 + T * 128), (u32)(cs * 4096 + qtr * 1024));
    } else {
      int li = n - 16, fs = li + rotf;
      if (fs >= 43) fs -= 43;
      int slot = li <= 22 ? 16 + li : li - 23;
      glds(rw2, v_w2, (u32)(fs * 32 * 4096 + st * 128), (u32)(slot * 4096 + qtr * 1024));
    }
  };
  u32 tl, th;
  int f_next = 0;
  auto make_tag = [&]() {
    const v2u ev = *(LAS v2u*)(L_MISC + 512);
    u64 e = ((u64)__builtin_amdgcn_readfirstlane(ev.y) << 32) | (u32)__builtin_amdgcn_readfirstlane(ev.x);
    e = e * 6364136223846793005ull + 1442695040888963407ull;
    tl = (u32)e;
    th = (u32)(e >> 32);
  };

  if (isU) {
    const int cl = r / 3, dt = r - 3 * cl;
    const int col = (xcd >> 2) * 22 + ((xcd & 3) >> 1) * 11 + ((xcd & 1) ? 6 : 0) + cl;
    const int uidx = col * 3 + dt, par = col >= 22, line = col - par;
    const bool doLN = uidx < 128;
    const rsrc_t rw1 = mk(pw1, S * F * 2);
    v4u x0, x1;
    {
      u32 vo = doLN ? (u32)((uidx * 16 + 2 * w) * 1024 + lane * 16) : OOB;
      asm volatile("buffer_load_dwordx4 %0, %1, %2, 0 offen" : "=v"(x0) : "v"(vo), "s"(rx));
      asm volatile("buffer_load_dwordx4 %0, %1, %2, 0 offen offset:1024" : "=v"(x1) : "v"(vo), "s"(rx));
    }
    const u32 v_w1 = (u32)knat * 5504 + csrc;
    auto w1_issue = [&](int n) {
      int s = (n + 11 * dt) & 31;
      glds(rw1, v_w1, (u32)(s * 32 * 5504 + line * 128), (u32)(s * 4096 + qtr * 1024));
    };
#pragma unroll
    for (int j = 0; j < 4; ++j) w1_issue((w >> 2) * 4 + j);
    asm volatile("s_waitcnt vmcnt(4)" : "+v"(x0), "+v"(x1)::"memory");
    STAMP(1);
    {
      float a[2][8], m[2], rs[2];
#pragma unroll
      for (int j = 0; j < 4; ++j) {
        a[0][2 * j] = bflo(x0[j]); a[0][2 * j + 1] = bfhi(x0[j]);
        a[1][2 * j] = bflo(x1[j]); a[1][2 * j + 1] = bfhi(x1[j]);
      }
#pragma unroll
      for (int p = 0; p < 2; ++p) {
        float s = 0.f;
#pragma unroll
        for (int j = 0; j < 8; ++j) s += a[p][j];
        m[p] = wave_sum(s) * (1.f / D);
      }
#pragma unroll
      for (int p = 0; p < 2; ++p) {
        float s = 0.f;
#pragma unroll
        for (int j = 0; j < 8; ++j) { a[p][j] -= m[p]; s += a[p][j] * a[p][j]; }
        rs[p] = __builtin_amdgcn_rsqf(wave_sum(s) * (1.f / D) + 1e-5f);
      }
#pragma unroll
      for (int p = 0; p < 2; ++p) {
        float s1 = 0.f, s2 = 0.f;
#pragma unroll
        for (int j = 0; j < 4; ++j) {
          u32 t = pk(a[p][2 * j] * rs[p], a[p][2 * j + 1] * rs[p]);
          a[p][2 * j] = bflo(t); a[p][2 * j + 1] = bfhi(t);
        }
#pragma unroll
        for (int j = 0; j < 8; ++j) { s1 += a[p][j]; s2 += a[p][j] * a[p][j]; }
        float m2 = wave_sum(s1) * (1.f / D), q2 = wave_sum(s2) * (1.f / D);
        float r2 = __builtin_amdgcn_rsqf(fmaxf(q2 - m2 * m2, 0.f) + 1e-5f);
        v4u o;
#pragma unroll
        for (int j = 0; j < 4; ++j) o[j] = pk((a[p][2 * j] - m2) * r2, (a[p][2 * j + 1] - m2) * r2);
        *(LAS v4u*)(L_LN + (p * 8 + w) * 1088 + lane * 16) = o;
        if (lane == 0) *(LAS f2*)(L_LNST + (p * 8 + w) * 8) = f2{m[p], rs[p]};
      }
    }
    BAR();
    STAMP(2);
    make_tag();
    if (w < 4) {
      const int hp = w >> 1, hh = w & 1;
      if (doLN) {
        const u32 ta = L_LN + (u32)((hp * 8 + (i16 >> 2)) * 1088 + hh * 512 + g * 32 + (i16 & 3) * 8);
#pragma unroll
        for (int j = 0; j < 4; ++j) {
          v2u lo = trd(ta + j * 128), hi = trd(ta + j * 128 + 4 * 1088);
          st128(v4u{lo.x, lo.y, hi.x, hi.y}, ws, (u32)(lane * 16 + j * 1024), WS_H + (u32)((hp * 128 + uidx) * 8192 + hh * 4096));
        }
        if (w == 0 && lane < 8) {
          v4u sv = lds128(L_LNST + lane * 16);
          st128(sv, ws, (u32)((lane >> 2) * 8192 + (lane & 3) * 16), WS_STATS + (u32)(uidx * 64));
        }
        WAITV(0);
        post(ws, WS_HFLAG + (u32)((w * 128 + uidx) * 8), lane, tl, th);
      }
      if (w == 0) post(ws, WS_AFLAG + (u32)(b * 8), lane, tl, th);
      STAMP(3);
      const int db0 = dt * 11 + 3 * w, dbe = dt == 2 ? 32 : dt * 11 + 11;
      u32 vh[3];
#pragma unroll
      for (int j = 0; j < 3; ++j) vh[j] = db0 + j < dbe ? (u32)(g * 8192 + ((db0 + j) * 16 + i16) * 16) : OOB;
      const int fkeep = (2 * line) % 43, fsend = (2 * line + 1) % 43;
      const bool keep = col != 22, send = col != 21;
      const rsrc_t rb1 = mk(pb1, F * 2);
      v2u bia[2];
      bia[0] = __builtin_amdgcn_raw_buffer_load_b64(rb1, (u32)(g * 8), (u32)(fkeep * 64), 0);
      bia[1] = __builtin_amdgcn_raw_buffer_load_b64(rb1, (u32)(g * 8 + 32), (u32)(fkeep * 64), 0);
      poll256(ws, (u32)(lane * 16), WS_HFLAG + (u32)(par * 2048), tl, th);
      STAMP(4);
      constexpr int HW = 5;
      v4u hf[HW][3];
      const int rot = cl * 5;
      const u32 hb = WS_H + (u32)(par * 128 * 8192);
#pragma unroll
      for (int n = 0; n < HW; ++n)
#pragma unroll
        for (int j = 0; j < 3; ++j) hf[n][j] = ld128(ws, vh[j], hb + (u32)(((n + rot) & 31) * 32768));
      SCHED();
      asm volatile("s_barrier" ::: "memory");
      STAMP(5);
      f4 acc[3][4];
#pragma unroll
      for (int j = 0; j < 3; ++j)
#pragma unroll
        for (int c = 0; c < 4; ++c) acc[j][c] = f4{0.f, 0.f, 0.f, 0.f};
      v4u wf[2][4];
      {
        u32 a = wbase + (u32)((rot & 31) * 4096);
#pragma unroll
        for (int c = 0; c < 4; ++c) wf[0][c] = wfrag(a ^ (c << 5));
      }
#pragma unroll
      for (int n = 0; n < 32; ++n) {
        if (n + 1 < 32) {
          u32 a = wbase + (u32)(((n + 1 + rot) & 31) * 4096);
#pragma unroll
          for (int c = 0; c < 4; ++c) wf[(n + 1) & 1][c] = wfrag(a ^ (c << 5));
        }
#pragma unroll
        for (int j = 0; j < 3; ++j) {
#pragma unroll
          for (int c = 0; c < 4; ++c) acc[j][c] = mma(wf[n & 1][c], hf[n % HW][j], acc[j][c]);
          if (n + HW < 32) hf[n % HW][j] = ld128(ws, vh[j], hb + (u32)(((n + HW + rot) & 31) * 32768));
        }
        SCHED();
      }
      STAMP(6);
      u32 vr[3];
#pragma unroll
      for (int j = 0; j < 3; ++j) vr[j] = db0 + j < dbe ? (u32)((3 * w + j) * 2048 + lane * 16) : OOB;
      if (send) {
#pragma unroll
        for (int j = 0; j < 3; ++j) {
          st128(__builtin_bit_cast(v4u, acc[j][2]), ws, vr[j], WS_R + (u32)((dt * 43 + fsend) * 24576));
          st128(__builtin_bit_cast(v4u, acc[j][3]), ws, vr[j] + 1024, WS_R + (u32)((dt * 43 + fsend) * 24576));
        }
      }
      BAR();
      if (send) {
        WAITV(0);
        post(ws, WS_RFLAG + (u32)(((dt * 43 + fsend) * 4 + w) * 64), lane, tl, th);
      }
      STAMP(7);
      if (keep) {
        poll64(ws, 0u, WS_RFLAG + (u32)(((dt * 43 + fkeep) * 4 + w) * 64), true, tl, th);
        STAMP(8);
        v4u pr[3][2];
#pragma unroll
        for (int j = 0; j < 3; ++j) {
          pr[j][0] = ld128(ws, vr[j], WS_R + (u32)((dt * 43 + fkeep) * 24576));
          pr[j][1] = ld128(ws, vr[j] + 1024, WS_R + (u32)((dt * 43 + fkeep) * 24576));
        }
#pragma unroll
        for (int j = 0; j < 3; ++j) {
          v4u o;
#pragma unroll
          for (int t = 0; t < 2; ++t) {
            float y[4];
#pragma unroll
            for (int e = 0; e < 4; ++e) {
              float bb = (e & 1) ? bfhi(bia[t][e >> 1]) : bflo(bia[t][e >> 1]);
              float a = acc[j][t][e] + asf(pr[j][t][e]) + bb;
              float z = a * (-2.302208198f + -0.1029432397f * a * a);
              y[e] = a * __builtin_amdgcn_rcpf(1.f + __builtin_amdgcn_exp2f(z));
            }
            o[2 * t] = pk(y[0], y[1]);
            o[2 * t + 1] = pk(y[2], y[3]);
          }
          u32 vo = db0 + j < dbe ? (u32)((db0 + j) * 43 * 1024 + lane * 16) : OOB;
          st128(o, ws, vo, WS_U + (u32)(fkeep * 1024));
        }
        WAITV(0);
        post(ws, WS_UFLAG + (u32)(((dt * 4 + w) * 64 + fkeep) * 8), lane, tl, th);
      }
      STAMP(9);
    } else {
#pragma unroll 1
      for (int n = 8; n < 32; ++n) {
        w1_issue(n);
        WAITV(8);
      }
      WAITV(0);
      STAMP(3);
      asm volatile("s_barrier" ::: "memory");
      asm volatile("s_barrier" ::: "memory");
      STAMP(7);
    }
  } else if (isQ) {
    const int rt = qi >> 2, hgl = qi & 3, hg = (xcd >> 2) * 4 + hgl;
    const int c0 = 2 * ((hgl + rt) & 3);
    const rsrc_t rwq = mk(pwqkv, D * 1536 * 2);
    const u32 v_wq = (u32)(((w >> 2) * 32 + knat) * 3072) + csrc;
    u32 v_x[3];
#pragma unroll
    for (int j = 0; j < 3; ++j) {
      int R = 8 * (w + 8 * j) + (lane >> 3);
      int p = R >= 80, pos = R - 80 * p, row = 144 * rt - 16 + 2 * pos + p;
      v_x[j] = (R < 160 && row >= 0 && row < S) ? (u32)(row * 1024 + (((lane & 7) ^ ((R >> 1) & 7)) * 16)) : OOB;
    }
    const u32 x2dst = w < 4 ? (u32)(Q_X + (w + 16) * 1024) : 0u;
    auto q_issue = [&](int c) {
      const u32 sb = (u32)((c % 3) * Q_STAGE);
      const int ch = (c + c0) & 7;
#pragma unroll
      for (int j = 0; j < 3; ++j) {
        glds(rwq, v_wq, (u32)(ch * 64 * 3072 + j * 1024 + hg * 128), sb + (u32)(j * 8192 + w * 1024));
        glds(rx, v_x[j], (u32)(ch * 128), j < 2 ? sb + (u32)(Q_X + (w + 8 * j) * 1024) : (w < 4 ? sb + x2dst : L_QDUMMY));
      }
    };
    q_issue(0); q_issue(1); q_issue(2);
    const int hd = w & 3, par = w >> 2;
    const u32 wa = wbase ^ (u32)(hd << 5);
    const u32 xa = Q_X + (u32)(par * 10240 + i16 * 128 + ((g ^ (i16 >> 1)) << 4));
    f4 aq[5], ak[5], av[5];
#pragma unroll
    for (int j = 0; j < 5; ++j) aq[j] = ak[j] = av[j] = f4{0.f, 0.f, 0.f, 0.f};
    v4u xf[2][5], wf[2][3];
    auto q_read = [&](int set, int c, int step) {
      const u32 sb = (u32)((c % 3) * Q_STAGE);
#pragma unroll
      for (int m = 0; m < 3; ++m) wf[set][m] = wfrag(sb + wa + (u32)(m * 8192 + step * 4096));
#pragma unroll
      for (int j = 0; j < 5; ++j) xf[set][j] = lds128(sb + (xa ^ (u32)(step * 64)) + (u32)(j * 2048));
    };
    auto q_mma = [&](int set) {
      v4u x0 = xf[set][0];
#pragma unroll
      for (int e = 0; e < 4; ++e) x0[e] = i16 == 0 ? 0x3F803F80u : x0[e];
#pragma unroll
      for (int j = 4; j >= 0; --j) {
        v4u xx = j == 0 ? x0 : xf[set][j];
        aq[j] = mma(wf[set][0], xx, aq[j]);
        ak[j] = mma(wf[set][1], xx, ak[j]);
        av[j] = mma(xx, wf[set][2], av[j]);
      }
    };
    WAITV(12);
    BAR();
    STAMP(1);
    make_tag();
    if (w == 0) post(ws, WS_AFLAG + (u32)(b * 8), lane, tl, th);
    q_read(0, 0, 0);
    const int kb0 = 9 * rt - 1;
    const bool fact = lane < 10 && kb0 + lane >= 0 && kb0 + lane < 128;
    const u32 vfl = fact ? (u32)((kb0 + lane) * 8) : OOB;
    v2u fl, s0, s1;
#pragma unroll
    for (int c = 0; c < 8; ++c) {
      q_read(1, c, 1);
      SCHED();
      q_mma(0);
      SCHED();
      if (c < 7) {
        if (c <= 5) { WAITV(6); } else { asm volatile("s_waitcnt vmcnt(0)" : "+v"(fl)::"memory"); }
        BAR();
        if (c + 3 < 8) q_issue(c + 3);
        if (c == 5) asm volatile("buffer_load_dwordx2 %0, %1, %2, %3 offen sc1" : "=v"(fl) : "v"(vfl), "s"(ws), "s"(WS_HFLAG));
        if (c == 6) {
          if (!__all(!fact || (fl.x == tl && fl.y == th))) poll64(ws, vfl, WS_HFLAG, fact, tl, th);
          FENCE();
          const int p0 = 72 * rt - 8 + lane, p1 = p0 + 64;
          s0 = ld64(ws, (p0 >= 0 && p0 < 1024) ? (u32)(p0 * 8) : OOB, WS_STATS + (u32)(par * 8192));
          s1 = ld64(ws, (lane < 16 && p1 < 1024) ? (u32)(p1 * 8) : OOB, WS_STATS + (u32)(par * 8192));
        }
        q_read(0, c + 1, 0);
        SCHED();
      }
      q_mma(1);
      SCHED();
    }
    STAMP(2);
    const u32 sta = L_QST2 + (u32)(w * 640);
    *(LAS v2u*)(sta + lane * 8) = s0;
    if (lane < 16) *(LAS v2u*)(sta + 512 + lane * 8) = s1;
    BAR();
    if (w >= 4) {
#pragma unroll 1
      for (; f_next < 20; ++f_next) f_issue(f_next);
    }
    STAMP(3);
    float cq[4], ck[4];
#pragma unroll
    for (int e = 0; e < 4; ++e) { cq[e] = dpp<0x150>(aq[0][e]); ck[e] = dpp<0x150>(ak[0][e]); }
    const float cv = __shfl(av[0][0], i16);
    v2u qf[5], kf[5], vf[5];
#pragma unroll
    for (int j = 0; j < 5; ++j) {
      f2 sp = *(LAS f2*)(sta + (j * 16 + i16) * 8);
      f4 sa = *(LAS f4*)(sta + (j * 16 + 4 * g) * 8), sb = *(LAS f4*)(sta + (j * 16 + 4 * g) * 8 + 16);
      float t[4];
#pragma unroll
      for (int e = 0; e < 4; ++e) t[e] = (aq[j][e] - sp.x * cq[e]) * sp.y;
      qf[j] = v2u{pk(t[0], t[1]), pk(t[2], t[3])};
#pragma unroll
      for (int e = 0; e < 4; ++e) t[e] = (ak[j][e] - sp.x * ck[e]) * sp.y;
      kf[j] = v2u{pk(t[0], t[1]), pk(t[2], t[3])};
      t[0] = (av[j][0] - sa.x * cv) * sa.y; t[1] = (av[j][1] - sa.z * cv) * sa.w;
      t[2] = (av[j][2] - sb.x * cv) * sb.y; t[3] = (av[j][3] - sb.z * cv) * sb.w;
      vf[j] = v2u{pk(t[0], t[1]), pk(t[2], t[3])};
    }
    bool cur[4], val[4];
#pragma unroll
    for (int e = 0; e < 4; ++e) {
      int key = 4 * g + e;
      cur[e] = key <= i16;
      val[e] = cur[e] ? key >= i16 - 7 : key >= i16 + 9;
    }
    const u32 mc0 = (cur[0] ? 0xffffu : 0u) | (cur[1] ? 0xffff0000u : 0u), mc1 = (cur[2] ? 0xffffu : 0u) | (cur[3] ? 0xffff0000u : 0u);
    const f4 z4 = {0.f, 0.f, 0.f, 0.f};
#pragma unroll
    for (int j = 0; j < 5; ++j) {
      f4 sc = mma16(kf[j], qf[j], z4), sp = z4;
      if (j > 0) sp = mma16(kf[j - 1], qf[j], z4);
      float s[4];
#pragma unroll
      for (int e = 0; e < 4; ++e) {
        bool v = val[e];
        if (j == 0) v = v && cur[e] && (rt > 0 || 4 * g + e >= 8 || i16 < 8);
        s[e] = v ? (cur[e] ? sc[e] : sp[e]) : -3.0e38f;
      }
      float mx = max_groups(fmaxf(fmaxf(s[0], s[1]), fmaxf(s[2], s[3])));
      const float cs = 0.25f * 1.44269504f;
      float nm = -mx * cs, p[4];
#pragma unroll
      for (int e = 0; e < 4; ++e) p[e] = __builtin_amdgcn_exp2f(fmaxf(s[e] * cs + nm, -200.f));
      float inv = __builtin_amdgcn_rcpf(sum_groups((p[0] + p[1]) + (p[2] + p[3])));
      u32 p0 = pk(p[0] * inv, p[1] * inv), p1 = pk(p[2] * inv, p[3] * inv);
      f4 o;
      if (j == 0) o = mma16(vf[0], v2u{p0 & mc0, p1 & mc1}, z4);
      else o = mma(v4u{vf[j - 1].x, vf[j - 1].y, vf[j].x, vf[j].y}, v4u{p0 & ~mc0, p1 & ~mc1, p0 & mc0, p1 & mc1}, z4);
      int rl = 2 * (j * 16 + i16 - 8) + par;
      if (rl >= 0) *(LAS v2u*)(L_OST + (u32)(((rl >> 4) * 2 + (hd >> 1)) * 1024 + (g * 16 + (rl & 15)) * 16 + (hd & 1) * 8)) = v2u{pk(o[0], o[1]), pk(o[2], o[3])};
    }
    BAR();
    STAMP(4);
    if (w < 4) {
#pragma unroll
      for (int j = 0; j < 3; ++j) {
        int sbl = w + 4 * j, sbg = 9 * rt + sbl;
        if (sbl < 9 && sbg < 128) {
          v4u o0 = lds128(L_OST + (u32)(sbl * 2048 + lane * 16)), o1 = lds128(L_OST + (u32)(sbl * 2048 + 1024 + lane * 16));
          st128(o0, ws, (u32)(lane * 16), WS_O + (u32)((sbg * 16 + hg * 2) * 1024));
          st128(o1, ws, (u32)(lane * 16 + 1024), WS_O + (u32)((sbg * 16 + hg * 2) * 1024));
        }
      }
    }
    BAR();
    if (w < 4) {
      WAITV(0);
      post(ws, WS_OFLAG + (u32)(((rt * 4 + w) * 8 + hg) * 8), lane, tl, th);
    }
    STAMP(7);
  } else {
    WAITV(0);
    BAR();
    make_tag();
    if (w == 0) {
      post(ws, WS_AFLAG + (u32)(b * 8), lane, tl, th);
      if (b == 249) {
        poll256(ws, (u32)(lane * 16), WS_AFLAG, tl, th);
        post(ws, WS_E, lane, tl, th);
      }
    }
  }

  v4u pre[4][4];
  f4 ao[4];
  const int dg = w & 3;
  const u32 slot0 = w >= 4 ? 16u * 4096u : 0u;
  if (w >= 4) {
    const int sbg = 4 * st + dg, ort = sbg / 9, osb = sbg - 9 * ort;
    const u32 vof = lane < 8 ? (u32)(lane * 8) : OOB;
    const u32 sof = WS_OFLAG + (u32)((ort * 4 + (osb & 3)) * 64);
#pragma unroll 1
    for (; f_next < 23; ++f_next) { f_issue(f_next); WAITV(8); }
    v2u fl;
    asm volatile("buffer_load_dwordx2 %0, %1, %2, %3 offen sc1" : "=v"(fl) : "v"(vof), "s"(ws), "s"(sof));
#pragma unroll 1
    for (; f_next < 30; ++f_next) { f_issue(f_next); WAITV(8); }
    f_issue(30);
    asm volatile("s_waitcnt vmcnt(8)" : "+v"(fl)::"memory");
    if (!__all(lane >= 8 || (fl.x == tl && fl.y == th))) poll64(ws, vof, sof, lane < 8, tl, th);
    FENCE();
    STAMP(8);
    v4u of[16];
    const u32 vo = (u32)(lane * 16), so = WS_O + (u32)(sbg * 16384);
#pragma unroll
    for (int c = 0; c < 16; ++c)
      asm volatile("buffer_load_dwordx4 %0, %1, %2, %3 offen offset:%4 sc1" : "=v"(of[c]) : "v"(vo), "s"(ws), "s"(so + (u32)((c >> 2) * 4096)), "i"((c & 3) * 1024));
#pragma unroll 1
    for (int n = 31; n < 39; ++n) f_issue(n);
    hbar(4, lane);
    asm volatile("s_waitcnt vmcnt(8)"
                 : "+v"(of[0]), "+v"(of[1]), "+v"(of[2]), "+v"(of[3]), "+v"(of[4]), "+v"(of[5]), "+v"(of[6]), "+v"(of[7]), "+v"(of[8]),
                   "+v"(of[9]), "+v"(of[10]), "+v"(of[11]), "+v"(of[12]), "+v"(of[13]), "+v"(of[14]), "+v"(of[15])::"memory");
    STAMP(9);
#pragma unroll
    for (int c = 0; c < 4; ++c) ao[c] = f4{0.f, 0.f, 0.f, 0.f};
#pragma unroll
    for (int c = 0; c < 16; ++c)
#pragma unroll
      for (int d = 0; d < 4; ++d) ao[d] = mma(wfrag((wbase ^ (u32)(d << 5)) + (u32)(c * 4096)), of[c], ao[d]);
    SCHED();
#pragma unroll
    for (int n = 0; n < 4; ++n)
#pragma unroll
      for (int c = 0; c < 4; ++c) pre[n][c] = wfrag((wbase ^ (u32)(c << 5)) + slot0 + (u32)(n * 4096));
    hbar(8, lane);
    STAMP(10);
#pragma unroll 1
    for (int n = 39; n < 59; ++n) { f_issue(n); WAITV(8); }
    WAITV(0);
    STAMP(11);
  } else {
    const int db = 4 * T + dg, udt = db / 11, udg = (db - 11 * udt) / 3;
    poll64(ws, lane < 43 ? (u32)(lane * 8) : OOB, WS_UFLAG + (u32)((udt * 4 + udg) * 512), lane < 43, tl, th);
    STAMP(11);
#pragma unroll
    for (int c = 0; c < 4; ++c) ao[c] = f4{0.f, 0.f, 0.f, 0.f};
  }
  asm volatile("s_barrier" ::: "memory");
  STAMP(12);
  {
    const int db = 4 * T + dg;
    const int l0 = (w >= 4 ? 0 : 23) + rotf;
    v4u uf[23];
    const u32 ub = WS_U + (u32)(db * 43 * 1024);
#pragma unroll
    for (int n = 0; n < 23; ++n) {
      int fs = l0 + n;
      fs = fs >= 86 ? fs - 86 : fs >= 43 ? fs - 43 : fs;
      uf[n] = ld128(ws, (n < 20 || w >= 4) ? (u32)(lane * 16) : OOB, ub + (u32)(fs * 1024));
    }
    const int row = 64 * st + (tid >> 3);
    const v4u xr = __builtin_amdgcn_raw_buffer_load_b128(rx, (u32)(row * 1024 + T * 128 + (tid & 7) * 16), 0, 0);
    const float b2 = bflo(((const unsigned short*)pb2)[row]);
    SCHED();
    if (w < 4) {
#pragma unroll
      for (int n = 0; n < 4; ++n)
#pragma unroll
        for (int c = 0; c < 4; ++c) pre[n][c] = wfrag((wbase ^ (u32)(c << 5)) + (u32)(n * 4096));
    }
    f4 acc[4];
#pragma unroll
    for (int c = 0; c < 4; ++c) acc[c] = f4{0.f, 0.f, 0.f, 0.f};
#pragma unroll
    for (int n = 0; n < 20; ++n) {
#pragma unroll
      for (int c = 0; c < 4; ++c) {
        acc[c] = mma(uf[n], pre[n & 3][c], acc[c]);
        if (n + 4 < 23) pre[n & 3][c] = wfrag((wbase ^ (u32)(c << 5)) + slot0 + (u32)((n + 4) * 4096));
      }
    }
    if (w >= 4) {
#pragma unroll
      for (int n = 20; n < 23; ++n)
#pragma unroll
        for (int c = 0; c < 4; ++c) acc[c] = mma(uf[n], pre[n & 3][c], acc[c]);
    }
    STAMP(13);
    BAR();
    const u32 rb = (w >= 4 ? 17408u : 0u) + (u32)(i16 * 272 + dg * 64 + g * 16);
#pragma unroll
    for (int c = 0; c < 4; ++c) *(LAS f4*)(rb + c * 16 * 272) = acc[c];
    if (w >= 4) {
#pragma unroll
      for (int c = 0; c < 4; ++c) *(LAS f4*)(2 * 17408 + (u32)((dg * 16 + i16) * 272 + c * 64 + g * 16)) = ao[c];
    }
    BAR();
    const u32 ra = (u32)((tid >> 3) * 272 + (tid & 7) * 32);
    f4 lo = *(LAS f4*)ra + *(LAS f4*)(ra + 17408) + *(LAS f4*)(ra + 2 * 17408);
    f4 hi = *(LAS f4*)(ra + 16) + *(LAS f4*)(ra + 17408 + 16) + *(LAS f4*)(ra + 2 * 17408 + 16);
    v4u o;
    o[0] = pk(lo[0] + bflo(xr[0]) + b2, lo[1] + bfhi(xr[0]) + b2);
    o[1] = pk(lo[2] + bflo(xr[1]) + b2, lo[3] + bfhi(xr[1]) + b2);
    o[2] = pk(hi[0] + bflo(xr[2]) + b2, hi[1] + bfhi(xr[2]) + b2);
    o[3] = pk(hi[2] + bflo(xr[3]) + b2, hi[3] + bfhi(xr[3]) + b2);
    *(v4u*)((char*)pout + (size_t)row * 1024 + T * 128 + (tid & 7) * 16) = o;
  }
  STAMP(14);
  asm volatile(".Lfk_end:" ::: "memory");
}

extern "C" void kernel_launch(void* const* d_in, const int* in_sizes, int n_in, void* d_out, int out_size, void* d_ws, size_t ws_size,
                              hipStream_t stream) {
  fused_kernel<<<256, 512, 0, stream>>>(d_in[0], d_in[1], d_in[2], d_in[4], d_in[5], d_in[6], d_in[7], d_out, d_ws);
}
```

```cpp
#include <hip/hip_runtime.h>
#pragma clang diagnostic ignored "-Wint-to-pointer-cast"

typedef unsigned u32;
typedef unsigned long long u64;
typedef u32 v4u __attribute__((ext_vector_type(4)));
typedef u32 v2u __attribute__((ext_vector_type(2)));
typedef float f4 __attribute__((ext_vector_type(4)));
typedef float f2 __attribute__((ext_vector_type(2)));
typedef short s4 __attribute__((ext_vector_type(4)));
typedef __bf16 bf8 __attribute__((ext_vector_type(8)));
typedef __bf16 bf2 __attribute__((ext_vector_type(2)));
typedef __amdgpu_buffer_rsrc_t rsrc_t;
#define LAS __attribute__((address_space(3)))
#define DI __device__ __forceinline__

constexpr int S = 2048, D = 512, F = 1376;
constexpr u32 WS_E = 0, WS_AFLAG = 4096  , WS_HFLAG = 32768  ,
              WS_STATS = 81920, WS_H = 1u << 20, WS_O = 3u << 20, WS_U = 5u << 20, WS_R = 7u << 20, WS_END = 11u << 20;
constexpr u32 WS_STAMP = 16u << 20;
constexpr u32 OOB = 0x80000000u, OOBS = 0x40000000u;
constexpr int SC1 = 16;
constexpr u32 LDS_BYTES = 163840;
constexpr u32 L_MISC = 39 * 4096;
constexpr u32 L_LN = 131072;
constexpr u32 C_HB = L_MISC, C_H = L_MISC + 4, C_GO = L_MISC + 8, C_W = L_MISC + 12  , C_A0 = L_MISC + 24, C_A1 = L_MISC + 28,
              C_B0 = L_MISC + 32, C_B1 = L_MISC + 36, C_PRE = L_MISC + 40, C_Q6 = L_MISC + 44, C_Q7 = L_MISC + 48, C_LN = L_MISC + 44  ,
              C_DONE = L_MISC + 52, L_E = L_MISC + 512, L_B1 = L_MISC + 2048;
constexpr u32 SENT = 0xFFFFFFFFu;
#define PACE 5
#define FMAP 1
#define HOLD 12
#define PACE0 3
#define HROT 0
#define RWAIT 0
#define PACE2 6
constexpr u32 Q_STAGE = 45056, Q_X = 24576;
constexpr u32 L_QST = 3 * Q_STAGE;
constexpr u32 L_OST = L_MISC - 18 * 1024;
static_assert(L_QST + 2048 + 2048 <= L_OST && PACE0 <= PACE && PACE < 8, "map");
#define DBG 0
constexpr u32 SPIN_CAP = (DBG & 4) ? 1 : 1u << 18;
#define STAMPS 0
#define SCHED() __builtin_amdgcn_sched_barrier(0)
#define STAMP(i) do { } while (0)
#define GNOP "s_nop 0"
#define CNT(i) do { } while (0)

DI rsrc_t mk(const void* p, u32 n) { return __builtin_amdgcn_make_buffer_rsrc((void*)p, (short)0, (int)n, 0x00020000); }
template <int LOFF, int GOFF> DI void glds(rsrc_t rs, u32 voff, u32 soff, u32 lds) {
  if (DBG & 32) { asm volatile("" ::: "memory"); return; }
  asm volatile("s_add_u32 m0, %3, %4\n\t" GNOP "\n\tbuffer_load_dwordx4 %0, %1, %2 offen offset:%5 lds"
               ::"v"(voff), "s"(rs), "s"(soff), "s"(lds), "i"(LOFF - GOFF), "i"(GOFF) : "memory", "scc");
}
#define WAITV_(n) asm volatile("s_waitcnt vmcnt(" #n ")" ::: "memory")
#define WAITV(n) WAITV_(n)
#define BAR() asm volatile("s_waitcnt lgkmcnt(0)\n\ts_barrier" ::: "memory")
#define BAR0() asm volatile("s_barrier" ::: "memory")
#define FENCE() asm volatile("" ::: "memory")
#define NAP() asm volatile("s_sleep 4" ::: "memory")
DI v4u ld128(rsrc_t rs, u32 voff, u32 soff) { return __builtin_amdgcn_raw_buffer_load_b128(rs, voff, soff, SC1); }
DI v2u ld64(rsrc_t rs, u32 voff, u32 soff) { return __builtin_amdgcn_raw_buffer_load_b64(rs, voff, soff, SC1); }
DI void st128(v4u v, rsrc_t rs, u32 voff) { __builtin_amdgcn_raw_buffer_store_b128(v, rs, voff, 0, SC1); }
DI void st64(v2u v, rsrc_t rs, u32 voff) { __builtin_amdgcn_raw_buffer_store_b64(v, rs, voff, 0, SC1); }
DI float asf(u32 x) { return __builtin_bit_cast(float, x); }
DI u32 asu(float x) { return __builtin_bit_cast(u32, x); }
DI float bflo(u32 x) { return asf(x << 16); }
DI float bfhi(u32 x) { return asf(x & 0xffff0000u); }
DI u32 pk(float a, float b) { f2 p = {a, b}; bf2 q = __builtin_convertvector(p, bf2); return __builtin_bit_cast(u32, q); }
DI f4 mma(v4u a, v4u b, f4 c) {
  return __builtin_amdgcn_mfma_f32_16x16x32_bf16(__builtin_bit_cast(bf8, a), __builtin_bit_cast(bf8, b), c, 0, 0, 0);
}
DI f4 mma16(v2u a, v2u b, f4 c) {
  return __builtin_amdgcn_mfma_f32_16x16x16bf16_1k(__builtin_bit_cast(s4, a), __builtin_bit_cast(s4, b), c, 0, 0, 0);
}
DI v2u trd(u32 a) { return __builtin_bit_cast(v2u, __builtin_amdgcn_ds_read_tr16_b64_v4i16((LAS s4*)a)); }
DI void whalf(v4u& f, int h, u32 a) { v2u t = trd(a + 2048 * h); f[2 * h] = t.x; f[2 * h + 1] = t.y; }
DI v4u wfrag(u32 a) { v2u lo = trd(a), hi = trd(a + 2048); return v4u{lo.x, lo.y, hi.x, hi.y}; }
DI v4u lds128(u32 a) { return *(LAS v4u*)a; }
template <int CTRL> DI float dpp(float v) { return asf((u32)__builtin_amdgcn_update_dpp(0, (int)asu(v), CTRL, 0xf, 0xf, true)); }
DI float fmx(float a, float b) { return __builtin_elementwise_maximum(a, b); }
DI float row_sum(float v) {
  v += dpp<0xB1>(v);
  v += dpp<0x4E>(v);
  v += dpp<0x141>(v);
  v += dpp<0x140>(v);
  return v;
}
DI u32 umax3(u32 a, u32 b, u32 c) { return max(max(a, b), c); }
DI u32 smax(v4u v, u32 m) { return umax3(umax3(v.x, v.y, v.z), v.w, m); }
DI bool stale(u32 m) { return __any(m == SENT); }
DI bool tagged(v4u f, u32 tl, u32 th) { return f.x == tl && f.y == th && f.z == tl && f.w == th; }
DI void poll_flags(rsrc_t ws, u32 soff, int lane, int n2, u32 tl, u32 th) {
#pragma unroll 1
  for (u32 n = 0; n < SPIN_CAP; ++n) {
    v4u f = ld128(ws, (u32)(lane * 16), soff);
    bool ok = tagged(f, tl, th);
    if (n2) { v4u g = ld128(ws, lane < n2 ? (u32)(1024 + lane * 16) : OOB, soff); ok = ok && (lane >= n2 || tagged(g, tl, th)); }
    if (__all(ok)) break;
    NAP();
  }
  FENCE();
}
DI void post(rsrc_t ws, u32 base, int idx, int lane, u32 tl, u32 th) {
  if (lane < 8) st64(v2u{tl, th}, ws, base + (u32)(lane * 2048 + idx * 8));
}
DI u32 arrive(u32 addr) {
  u32 old;
  asm volatile("s_mov_b64 exec, 1\n\tds_add_rtn_u32 %0, %1, %2\n\ts_mov_b64 exec, -1\n\ts_waitcnt lgkmcnt(0)" : "=&v"(old) : "v"(addr), "v"(1u) : "memory");
  return __builtin_amdgcn_readfirstlane(old);
}
DI void lpost(u32 addr) {
  asm volatile("s_waitcnt lgkmcnt(0)\n\ts_mov_b64 exec, 1\n\tds_add_u32 %0, %1\n\ts_mov_b64 exec, -1" ::"v"(addr), "v"(1u) : "memory");
}
DI void lwait(u32 addr, u32 target) {
#pragma unroll 1
  for (u32 n = 0; n < (1u << 26); ++n) {
    if (__builtin_amdgcn_readfirstlane(__hip_atomic_load((LAS u32*)addr, __ATOMIC_RELAXED, __HIP_MEMORY_SCOPE_WORKGROUP)) >= target) break;
    asm volatile("s_nop 7" ::: "memory");
  }
  FENCE();
}
DI void hbar(u32 target) { lpost(L_MISC); lwait(L_MISC, target); }

template <int NB> DI void u_loop(f4 (&acc)[3][4], rsrc_t ws, u32 vh, u32 hb, u32 wbase, int rot) {
  v4u hf[4][NB], wf[2][4];
#pragma unroll
  for (int k = 0; k < 4; ++k)
#pragma unroll
    for (int j = 0; j < NB; ++j) { hf[k][j] = ld128(ws, vh + j * 256, hb + (u32)((rot + k) * 32768)); SCHED(); }
  u32 wa[4];
#pragma unroll
  for (int c = 0; c < 4; ++c) { wa[c] = (wbase ^ (u32)(c << 5)) + (u32)(rot * 4096); wf[0][c] = wfrag(wa[c]); }
  SCHED();
  auto group = [&](int nx, bool more) {
    const u32 so = hb + (u32)(nx * 32768);
    u32 wn[4];
#pragma unroll
    for (int k = 0; k < 4; ++k) {
#pragma unroll
      for (int m = 0; m < 4 * NB; ++m) {
        const int j = m >> 2, c = m & 3;
        acc[j][c] = mma(wf[k & 1][c], hf[k][j], acc[j][c]);
        if (k == 2 && m < 4) wn[m] = (wbase ^ (u32)(m << 5)) + (u32)(nx * 4096);
        if (m < 8 && (more || k < 3)) whalf(wf[(k + 1) & 1][m >> 1], m & 1, k < 3 ? wa[m >> 1] + (u32)((k + 1) * 4096) : wn[m >> 1]);
        if (c == 3 && more) hf[k][j] = ld128(ws, vh + j * 256, so + (u32)(k * 32768));
        SCHED();
      }
    }
#pragma unroll
    for (int c = 0; c < 4; ++c) wa[c] = wn[c];
  };
#pragma unroll 1
  for (int n = 1; n < 8; ++n) {
    if ((n & 1) == 0) lwait(L_MISC + 8 + 2 * n, 4);
    group((rot + 4 * n) & 31, true);
  }
  group(0, false);
}

__global__ __launch_bounds__(512, 2) void fused_kernel(const void* __restrict__ px, const void* __restrict__ pwqkv, const void* __restrict__ pwo,
                                                     const void* __restrict__ pw1, const void* __restrict__ pb1, const void* __restrict__ pw2,
                                                     const void* __restrict__ pb2, void* __restrict__ pout, void* __restrict__ pws) {
  __shared__ __attribute__((aligned(1024))) char lds_mem[LDS_BYTES];
  asm volatile("" ::"v"((u32)(size_t)(LAS char*)lds_mem));
  STAMP(0);
  const int tid = threadIdx.x, lane = tid & 63;
  const int b = blockIdx.x, xcd = b & 7, r = b >> 3;
  {
    u64 pc;
    u32 len;
    asm volatile("s_getpc_b64 %0\n.Lfk_pc:\n\ts_mov_b32 %1, .Lfk_end-.Lfk_pc" : "=s"(pc), "=s"(len));
    const rsrc_t rc = mk((const void*)pc, len);
    const u32 vo = (tid & 31) == 0 ? (u32)(((tid >> 5) * 32 + r) * 128) : OOB;
    asm volatile("s_mov_b32 m0, %2\n\ts_nop 0\n\tbuffer_load_dword %0, %1, 0 offen lds" ::"v"(vo), "s"(rc), "s"(L_MISC + 1024) : "memory");
  }
  const int w = __builtin_amdgcn_readfirstlane(tid >> 6);
  const int g = lane >> 4, i16 = lane & 15;
  const int odd = xcd & 1, nU = odd ? 15 : 18;
  int role = r < nU ? 0 : (r < 31 || !odd) ? 1 : 2;
  const int cl = (r * 11) >> 5, dt = r - 3 * cl;
  const int col = (xcd >> 1) * 11 + odd * 6 + cl;
  const int uidx = (xcd >> 1) * 33 + odd * 18 + r;
  const int kb = (xcd & 2) * 33 + odd * 36 + (xcd >> 2) * nU + r;
  const int qi = (xcd & 2) * 15 + odd * 17 + r - 18;
  const int rt = qi >> 2, hgl = qi & 3, hg = (xcd & 4) + hgl;
  const int qtr = w & 3;
  const int rho = ((tid >> 3) & 31);
  const u32 csrc = (u32)(((lane & 7) ^ (rho & 6)) * 16);
  const int knat = ((rho >> 2) & 3) * 8 + (rho >> 4) * 4 + (rho & 3);
  asm volatile("" ::"s"(px), "s"(pwqkv), "s"(pwo), "s"(pw1), "s"(pb1), "s"(pw2), "s"(pb2), "s"(pout), "s"(pws));
  const rsrc_t ws = mk(pws, WS_END), rx = mk(px, S * D * 2);

  const rsrc_t rwq = mk(pwqkv, D * 1536 * 2);
  u32 v_wq = 0, v_x[3] = {0, 0, 0};
  const int c0 = 2 * ((hgl + rt) & 3);
  const u32 wl = (u32)(w * 1024);
  const int rot = 4 * ((3 * dt + HROT * cl) & 7);
#define Q_PIECE(C, K)                                                                                      \
  do {                                                                                                     \
    const int ch_ = ((C) + c0) & 7;                                                                        \
    constexpr int sb_ = ((C) % 3) * Q_STAGE, j_ = (K) >> 1;                                                \
    if (((K) & 1) == 0) glds<sb_ + j_ * 8192, j_ * 1024>(rwq, v_wq, (u32)(ch_ * 64 * 3072), wl);           \
    else if (j_ < 2 || w < 4) glds<sb_ + Q_X + j_ * 8192, 0>(rx, v_x[j_], (u32)(ch_ * 128), wl);           \
  } while (0)
  if (role == 1) {
    v_wq = (u32)(((tid >> 8) * 32 + knat) * 3072 + hg * 128) + csrc;
    Q_PIECE(0, 0); Q_PIECE(0, 2); Q_PIECE(0, 4);
#pragma unroll
    for (int j = 0; j < 3; ++j) {
      const int R = (tid >> 3) + 64 * j;
      const int row = 144 * rt - 16 + 2 * R - (R >= 80 ? 159 : 0);
      v_x[j] = (u32)row < (u32)S ? (u32)(row * 1024 + (((lane & 7) ^ ((R >> 1) & 7)) * 16)) : OOB;
    }
    Q_PIECE(0, 1); Q_PIECE(0, 3); Q_PIECE(0, 5);
  }
  if (w == 7 && role != 0)
    asm volatile("s_mov_b32 m0, %2\n\ts_nop 0\n\tbuffer_load_dword %0, %1, 0 offen sc1 lds" ::"v"(lane < 2 ? (u32)(lane * 4) : OOB), "s"(ws), "s"(L_MISC + 512) : "memory");
  if (tid < 4) *(LAS v4u*)(L_MISC + tid * 16) = v4u{0u, 0u, 0u, 0u};
  asm volatile("" : "+s"(role));
  SCHED();

  v4u xv[4];
  v2u ev = {0u, 0u};
  const bool uchain = role == 0 && w < 4;
  if (uchain) {
    const u32 vx = kb < 128 ? (u32)((kb * 16 + 8 * (w & 1) + 2 * g + (w >> 1)) * 1024 + i16 * 16) : OOB;
#pragma unroll
    for (int j = 0; j < 4; ++j)
      asm volatile("buffer_load_dwordx4 %0, %1, %2, 0 offen offset:%3" : "=v"(xv[j]) : "v"(vx), "s"(rx), "i"(j * 256));
    asm volatile("buffer_load_dwordx2 %0, %1, %2, 0 offen sc1" : "=v"(ev) : "v"(w == 0 ? 0u : OOB), "s"(ws));
    BAR();
    const u32 ha = L_LN + (u32)(((w >> 1) * 8 + (w & 1) * 4 + g) * 1088 + i16 * 16);
    const u32 vst = (kb < 128 && i16 == 0) ? WS_STATS + (u32)((w >> 1) * 8192 + kb * 64 + (w & 1) * 32 + g * 8) : OOB;
    const u32 ta = L_LN + (u32)(((w >> 1) * 8 + (i16 >> 2)) * 1088 + (w & 1) * 512 + g * 32 + (i16 & 3) * 8);
    const u32 vo = kb < 128 ? WS_H + (u32)(((w >> 1) * 128 + kb) * 8192 + (w & 1) * 4096 + lane * 16) : OOB;
    asm volatile("s_waitcnt vmcnt(0)" : "+v"(xv[0]), "+v"(xv[1]), "+v"(xv[2]), "+v"(xv[3]), "+v"(ev)::"memory");
    STAMP(1);
    if (w == 0) *(LAS v2u*)L_E = ev;
    f2 a[16];
#pragma unroll
    for (int j = 0; j < 16; ++j) a[j] = f2{bflo(xv[j >> 2][j & 3]), bfhi(xv[j >> 2][j & 3])};
    f2 t0 = a[0] + a[1], t1 = a[2] + a[3];
#pragma unroll
    for (int j = 4; j < 16; j += 2) { t0 += a[j]; t1 += a[j + 1]; }
    t0 += t1;
    const float m = row_sum(t0.x + t0.y) * (1.f / D);
#pragma unroll
    for (int j = 0; j < 16; ++j) a[j] -= m;
    t0 = a[0] * a[0]; t1 = a[1] * a[1];
#pragma unroll
    for (int j = 2; j < 16; j += 2) { t0 += a[j] * a[j]; t1 += a[j + 1] * a[j + 1]; }
    t0 += t1;
    const float var = row_sum(t0.x + t0.y) * (1.f / D);
    const float rs = __builtin_amdgcn_rsqf(var + 1e-5f);
    const float sc = rs * __builtin_amdgcn_rsqf(var * rs * rs + 1e-5f);
#pragma unroll
    for (int j = 0; j < 4; ++j) {
      v4u o;
#pragma unroll
      for (int e = 0; e < 4; ++e) { const f2 y = a[4 * j + e] * sc; o[e] = pk(y.x, y.y); }
      *(LAS v4u*)(ha + j * 256) = o;
    }
    lpost(C_LN + (w >> 1) * 4);
    st64(v2u{asu(m), asu(rs)}, ws, vst);
    lwait(C_LN + (w >> 1) * 4, 2);
    STAMP(2);
#pragma unroll
    for (int j = 0; j < 4; ++j) {
      v2u lo = trd(ta + j * 128), hi = trd(ta + j * 128 + 4 * 1088);
      st128(v4u{lo.x, lo.y, hi.x, hi.y}, ws, vo + j * 1024);
    }
    SCHED();
  }
  const v4u sent4 = {SENT, SENT, SENT, SENT};
  const u32 wbase = (u32)((lane >> 2) * 128 + ((lane >> 3) & 3) * 32 + (lane & 3) * 8);
  const int st = 4 * xcd + (r & 3), T = ((r >> 2) + 2 * r) & 7;
  const int vy = (r >> 2) & 1, vz = (r >> 3) & 1, vw = vz;
  const rsrc_t rw2 = mk(pw2, F * S * 2), rwo = mk(pwo, D * D * 2);
  const u32 v_w2 = (u32)(rho * 4096 + st * 128) + csrc, v_wo = (u32)(rho * 1024 + T * 128) + csrc;
  const u32 ql = (u32)(qtr * 1024);
  u32 t_w2 = 0;
  const int fa0 = (vy ? 20 : 0) + (vz ? 11 : 0), fa1 = (vy ? 20 : 0) + (vz ? 0 : 12), fb0 = vy ? 0 : 23;
  const u32 v_w2a = v_w2 + (u32)(fa0 * 131072), v_w2h = v_w2 + (u32)(fa1 * 131072), v_w2b = v_w2 + (u32)(fb0 * 131072);
  auto wo_issue = [&](u32 t) { glds<0, 0>(rwo, v_wo, (t ^ (u32)(vw << 15)) << 3, ql + t); };
  auto w2a_issue = [&](u32 t) {
    if (t < 49152) glds<65536, 0>(rw2, v_w2a, t << 5, ql + t);
    else glds<65536, 0>(rw2, v_w2h, (t - 49152) << 5, ql + t);
  };
  auto w2h_issue = [&](u32 t) { glds<65536, 0>(rw2, v_w2h, (t - 49152) << 5, ql + t); };
  auto w2b_issue = [&](u32 t) { glds<0, 0>(rw2, v_w2b, t << 5, ql + t); };
  const int row = 64 * st + (tid >> 3);
  const u32 ov = (u32)(row * 1024 + T * 128 + (tid & 7) * 16);
  u32 tl, th;
  auto make_tag = [&]() {
    const v2u ev = *(LAS v2u*)L_E;
    u64 e = ((u64)__builtin_amdgcn_readfirstlane(ev.y) << 32) | (u32)__builtin_amdgcn_readfirstlane(ev.x);
    e = e * 6364136223846793005ull + 1442695040888963407ull;
    tl = (u32)e;
    th = (u32)(e >> 32);
  };
  const u32 rep = (u32)(xcd * 2048);

  if (role == 0) {
    const int par = col >= 22, line = col - par;
    int fkeep = 2 * line, fsend = 2 * line + 1;
    if (fkeep >= 43) fkeep -= 43;
    if (fsend >= 43) fsend -= 43;
    const bool keep = col != 22, send = col != 21;
    const int lb0 = (dt == 2 && qtr == 3) ? 8 : 3 * qtr;
    const bool three = qtr < 2 || (qtr == 2 && dt < 2);
    const u32 vr = (u32)(lb0 * 2048 + lane * 16);
    const u32 vs = send ? WS_R + (u32)((dt * 43 + fsend) * 24576) + vr : OOB;
    const u32 vu = keep ? WS_U + (u32)(((dt * 11 + lb0) * 43 + fkeep) * 1024 + lane * 16) : OOB;
    if (w < 4) {
      lwait(C_LN, 2);
      make_tag();
      WAITV(0);
      const bool last = arrive(C_H) == 3;
      if (last && kb < 128) post(ws, WS_HFLAG, kb, lane, tl, th);
      STAMP(3);
      if (last) {
        WAITV(0);
        poll_flags(ws, WS_HFLAG + rep, lane, 0, tl, th);
        lpost(C_GO);
      }
      STAMP(4);
      lwait(C_GO, 5);
      STAMP(5);
      f4 acc[3][4];
#pragma unroll
      for (int j = 0; j < 3; ++j)
#pragma unroll
        for (int c = 0; c < 4; ++c) acc[j][c] = f4{0.f, 0.f, 0.f, 0.f};
      const u32 vh = (u32)(g * 8192 + ((dt * 11 + lb0) * 16 + i16) * 16);
      const u32 hb = WS_H + (u32)(par * 128 * 8192);
      if (three) u_loop<3>(acc, ws, vh, hb, wbase, rot);
      else u_loop<2>(acc, ws, vh, hb, wbase, rot);
      STAMP(6);
      const v4u tg = {tl & 0xFFFu, (tl >> 12) & 0xFFFu, th & 0xFFFu, (th >> 12) & 0xFFFu};
#pragma unroll
      for (int j = 0; j < 3; ++j)
        if (j < 2 || three) {
          st128((__builtin_bit_cast(v4u, acc[j][2]) & ~0xFFFu) | tg, ws, vs + j * 2048);
          st128((__builtin_bit_cast(v4u, acc[j][3]) & ~0xFFFu) | tg, ws, vs + j * 2048 + 1024);
        }
      BAR();
      STAMP(7);
      if (keep) {
        v2u bia[2];
        bia[0] = *(LAS v2u*)(L_B1 + g * 8);
        bia[1] = *(LAS v2u*)(L_B1 + g * 8 + 32);
        float bb[8];
#pragma unroll
        for (int e = 0; e < 8; ++e) bb[e] = (e & 1) ? bfhi(bia[e >> 2][(e >> 1) & 1]) : bflo(bia[e >> 2][(e >> 1) & 1]);
#pragma unroll
        for (int j = 0; j < 3; ++j)
#pragma unroll
          for (int e = 0; e < 8; ++e) acc[j][e >> 2][e & 3] += bb[e];
        WAITV(RWAIT);
        v4u pr[3][2];
        const u32 v2 = three ? vr + 4096 : OOB;
#pragma unroll 1
        for (u32 n = 0; n < SPIN_CAP; ++n) {
          u32 m = 0;
#pragma unroll
          for (int j = 0; j < 3; ++j) {
            pr[j][0] = ld128(ws, j < 2 ? vr + j * 2048 : v2, WS_R + (u32)((dt * 43 + fkeep) * 24576));
            pr[j][1] = ld128(ws, (j < 2 ? vr + j * 2048 : v2) + 1024, WS_R + (u32)((dt * 43 + fkeep) * 24576));
          }
#pragma unroll
          for (int j = 0; j < 3; ++j) {
            const v4u d = (pr[j][0] ^ tg) | (pr[j][1] ^ tg);
            const u32 e = d.x | d.y | d.z | d.w;
            m |= (j < 2 || three) ? e : 0u;
          }
          CNT(10);
          if (!__any((m & 0xFFFu) != 0)) break;
          NAP();
        }
        STAMP(8);
#pragma unroll
        for (int j = 0; j < 3; ++j)
          if (j < 2 || three) {
            v4u o;
#pragma unroll
            for (int h = 0; h < 4; ++h) {
              const int t = h >> 1, e = (h & 1) * 2;
              const f2 a = f2{acc[j][t][e], acc[j][t][e + 1]} + f2{asf(pr[j][t][e]), asf(pr[j][t][e + 1])};
              const f2 z = a * (a * a * -0.1029432397f + -2.302208198f);
              const f2 d = f2{__builtin_amdgcn_exp2f(z.x), __builtin_amdgcn_exp2f(z.y)} + 1.f;
              const f2 y = a * f2{__builtin_amdgcn_rcpf(d.x), __builtin_amdgcn_rcpf(d.y)};
              o[h] = pk(y.x, y.y);
            }
            st128(o, ws, vu + j * 43 * 1024);
          }
      }
      STAMP(9);
    } else {
      const rsrc_t rw1 = mk(pw1, S * F * 2), rb1 = mk(pb1, F * 2);
      const u32 v_w1 = (u32)(knat * 5504 + line * 128) + csrc;
      u32 t1 = (u32)(rot * 4096);
      BAR();
      if (w == 4)
        asm volatile("s_mov_b32 m0, %3\n\ts_nop 0\n\tbuffer_load_dword %0, %1, %2 offen lds" ::"v"(lane < 16 ? (u32)(lane * 4) : OOB), "s"(rb1), "s"((u32)(fkeep * 64)), "s"(L_B1) : "memory");
#pragma unroll
      for (int n = 0; n < 32; ++n) {
        if (n == HOLD) lwait(C_H, 4);
        if (n < HOLD) { WAITV(PACE0); } else { WAITV(PACE); }
        if (n == 8 + PACE) lpost(C_GO);
        if (n == 16 + PACE) { lpost(C_W); STAMP(3); }
        if (n == 24 + PACE) lpost(C_W + 4);
        if ((n & 3) == 0) t1 &= 0x1FFFF;
        glds<0, 0>(rw1, v_w1, t1 * 43, ql + t1);
        t1 += 4096;
      }
      lwait(C_LN, 2);
      make_tag();
#pragma unroll
      for (int j = 0; j < 3; ++j)
        if (j < 2 || three) st128(sent4, ws, vu + j * 43 * 1024);
      if (kb < 128) {
#pragma unroll
        for (int j = 0; j < 4; ++j) st128(sent4, ws, WS_O + (u32)(kb * 16384 + qtr * 4096 + j * 1024 + lane * 16));
      }
      WAITV(0);
      if (arrive(C_W + 8) == 3) post(ws, WS_AFLAG, uidx, lane, tl, th);
      STAMP(4);
      BAR0();
      STAMP(7);
    }
  } else if (role == 1) {
    Q_PIECE(1, 0); Q_PIECE(1, 1); Q_PIECE(1, 2); Q_PIECE(1, 3); Q_PIECE(1, 4); Q_PIECE(1, 5);
    u32 vo[3];
#pragma unroll
    for (int j = 0; j < 3; ++j) {
      const int sbl = w + 4 * j, sbg = 9 * rt + sbl;
      vo[j] = (sbl < 9 && sbg < 128) ? WS_O + (u32)((sbg * 16 + hg * 2) * 1024 + lane * 16) : OOB;
    }
    const int hd = w & 3, par = w >> 2;
    u32 wa[2], xa[2][2];
    wa[0] = wbase ^ (u32)(hd << 5);
    xa[0][0] = Q_X + (u32)(par * 10240 + i16 * 128 + ((g ^ (i16 >> 1)) << 4));
    xa[0][1] = xa[0][0] ^ 64u;
    wa[1] = wa[0] + 65536; xa[1][0] = xa[0][0] + 65536; xa[1][1] = xa[0][1] + 65536;
    asm volatile("" : "+v"(wa[1]), "+v"(xa[1][0]), "+v"(xa[1][1]));
    f4 aq[5], ak[5], av[5];
#pragma unroll
    for (int j = 0; j < 5; ++j) aq[j] = ak[j] = av[j] = f4{0.f, 0.f, 0.f, 0.f};
    v4u xf[2][5], wf[2][3];
    auto q_read1 = [&](int set, int c, int step, int m) {
      if (m == 0 || m >= 7) {
        const int j = m == 0 ? 4 : 10 - m, o = (c % 3) * Q_STAGE + j * 2048;
        xf[set][j] = lds128(o < 65536 ? xa[0][step] + (u32)o : xa[1][step] + (u32)(o - 65536));
      } else {
        const int k = (m - 1) >> 1, h = (m - 1) & 1, o = (c % 3) * Q_STAGE + k * 8192 + step * 4096;
        whalf(wf[set][k], h, o + 2048 < 65536 ? wa[0] + (u32)o : wa[1] + (u32)(o - 65536));
      }
    };
#define Q_STEP(SET, RC, RSTEP, CI, K0, HOOK)                                                                    \
  _Pragma("unroll") for (int m = 0; m < 15; ++m) {                                                         \
    const int j = 4 - m / 3;                                                                               \
    if (m == 11) {                                                                                         \
           \
      _Pragma("unroll") for (int e = 0; e < 4; ++e) xf[SET][0][e] = i16 == 0 ? 0x3F803F80u : xf[SET][0][e]; \
    }                                                                                                      \
    if (m % 3 == 0) aq[j] = mma(wf[SET][0], xf[SET][j], aq[j]);                                            \
    else if (m % 3 == 1) ak[j] = mma(wf[SET][1], xf[SET][j], ak[j]);                                       \
    else av[j] = mma(xf[SET][j], wf[SET][2], av[j]);                                                       \
    if ((RC) < 8 && m < 11) q_read1(1 - (SET), RC, RSTEP, m);                                              \
    if ((CI) < 7 && m == 3) Q_PIECE((CI) & 7, K0);                                                         \
    if ((CI) < 7 && m == 8) Q_PIECE((CI) & 7, (K0) + 1);                                                   \
    if ((CI) < 7 && m == 13) Q_PIECE((CI) & 7, (K0) + 2);                                                  \
    if ((HOOK) == 9) {                                             \
      if (m == 1) Q_PIECE(7, 0); if (m == 3) Q_PIECE(7, 1); if (m == 5) Q_PIECE(7, 2);                     \
      if (m == 8) Q_PIECE(7, 3); if (m == 10) Q_PIECE(7, 4); if (m == 13) Q_PIECE(7, 5);                   \
    }                                                                                                      \
    q_hook(HOOK, m);                                                                                       \
    SCHED();                                                                                               \
  }
    f4 bc, bp;
#pragma unroll
    for (int e = 0; e < 4; ++e) {
      const int dk = i16 - (4 * g + e);
      bc[e] = (u32)dk < 8u ? 0.f : -1.0e30f;
      bp[e] = dk <= -9 ? 0.f : -1.0e30f;
    }
    const int dk0 = i16 - 4 * g;
    u32 mc0 = (dk0 >= 0 ? 0xffffu : 0u) | (dk0 >= 1 ? 0xffff0000u : 0u), mc1 = (dk0 >= 2 ? 0xffffu : 0u) | (dk0 >= 3 ? 0xffff0000u : 0u);
    const int rl = 2 * i16 - 16 + par;
    u32 oa = L_OST + (u32)((rl >> 4) * 2048 + (hd >> 1) * 1024 + g * 256 + (rl & 15) * 16 + (hd & 1) * 8);
    const u32 sta = L_QST + (u32)(par * 1024);
    u32 sw = sta + (u32)(lane * 4), sa1 = sta + (u32)(i16 * 4), sa4 = sta + (u32)(g * 16);
    const int p0 = 72 * rt - 8 + lane, p1 = p0 + 64;
    u32 vs0 = (u32)p0 < 1024u ? (u32)(p0 * 8) : OOB, vs1 = (lane < 16 && p1 < 1024) ? (u32)(p1 * 8) : OOB;
    asm volatile("" : "+v"(bc), "+v"(bp), "+v"(mc0), "+v"(mc1), "+v"(oa), "+v"(sw), "+v"(sa1), "+v"(sa4), "+v"(vs0), "+v"(vs1));
    WAITV(5);
    BAR();
    STAMP(1);
#pragma unroll
    for (int m = 0; m < 11; ++m) q_read1(0, 0, 0, m);
    Q_PIECE(2, 0); Q_PIECE(2, 1); Q_PIECE(2, 2);
    make_tag();
    v4u fa;
    v2u s0, s1;
    float m1[5], r1[5];
    f4 m4[5], r4[5];
    auto q_hook = [&](int hook, int m) {
      if (hook == 11 && m >= 6 && (m & 1) == 0 && w >= 4) w2a_issue((u32)((6 + (m - 6) / 2) * 4096));
      if (hook == 12 && m < 12 && (m & 1) == 1 && w >= 4) w2a_issue((u32)((11 + m / 2) * 4096));
      if (hook == 13 && m == 1 && w >= 4) lwait(C_Q6, 8);
      if ((hook == 13 || hook == 14) && m % 3 == 2 && w >= 4) wo_issue((u32)(((hook - 13) * 5 + m / 3) * 4096));
      if (hook == 15 && m == 2 && w >= 4) wo_issue(10 * 4096);
      if (hook == 10 && m == 0) asm volatile("buffer_load_dwordx4 %0, %1, %2, %3 offen sc1" : "=v"(fa) : "v"((u32)(lane * 16)), "s"(ws), "s"(WS_HFLAG + rep));
      if (hook == 11 && m == 1) {
        if (!__all(tagged(fa, tl, th))) poll_flags(ws, WS_HFLAG + rep, lane, 0, tl, th);
        FENCE();
      }
      if (hook == 11 && m == 3) asm volatile("buffer_load_dwordx2 %0, %1, %2, %3 offen sc1" : "=v"(s0) : "v"(vs0), "s"(ws), "s"(WS_STATS + (u32)(par * 8192)));
      if (hook == 11 && m == 5) asm volatile("buffer_load_dwordx2 %0, %1, %2, %3 offen sc1" : "=v"(s1) : "v"(vs1), "s"(ws), "s"(WS_STATS + (u32)(par * 8192)));
      if (hook == 5 && m == 1 && w == 0) post(ws, WS_AFLAG, 132 + (xcd >> 2) * 60 + qi, lane, tl, th);
      if (hook == 13 && m == 11) { *(LAS u32*)sw = s0.x; *(LAS u32*)(sw + 512) = s0.y; }
      if (hook == 13 && m == 12) { *(LAS u32*)(sw + 256) = s1.x; *(LAS u32*)(sw + 768) = s1.y; }
      if (hook == 15) {
        if (m < 5) { m1[m] = *(LAS float*)(sa1 + m * 64); r1[m] = *(LAS float*)(sa1 + 512 + m * 64); }
        else if (m < 10) { m4[m - 5] = *(LAS f4*)(sa4 + (m - 5) * 64); r4[m - 5] = *(LAS f4*)(sa4 + 512 + (m - 5) * 64); }
      }
    };
#define Q_CHUNK(C)                                                                                         \
  Q_STEP(0, C, 1, (C) + 2, 3, 2 * (C))                                                                     \
  if ((C) < 5) { WAITV(5); BAR(); }                                                                        \
  else if ((C) == 5) { asm volatile("s_waitcnt vmcnt(0)" : "+v"(fa)::"memory"); BAR(); }                   \
  else if ((C) == 6) { if (w < 4) { WAITV(0); } else { WAITV(11); } asm volatile("" : "+v"(s0), "+v"(s1)); lpost(C_Q6); } \
  else lpost(C_Q7);                                                                                 \
  SCHED();                                                                                                 \
  Q_STEP(1, (C) + 1, 0, (C) + 3, 0, 2 * (C) + 1)
#define QS(i) if (DBG & 64) STAMP(i)
    Q_CHUNK(0) QS(3); Q_CHUNK(1) QS(5); Q_CHUNK(2) QS(6); Q_CHUNK(3) QS(8); Q_CHUNK(4) QS(9); Q_CHUNK(5) QS(10); Q_CHUNK(6) QS(11); Q_CHUNK(7)
    STAMP(2);
    f2 cq[2], ck[2];
#pragma unroll
    for (int e = 0; e < 2; ++e) {
      cq[e] = f2{dpp<0x150>(aq[0][2 * e]), dpp<0x150>(aq[0][2 * e + 1])};
      ck[e] = f2{dpp<0x150>(ak[0][2 * e]), dpp<0x150>(ak[0][2 * e + 1])};
    }
    const float cv = asf((u32)__builtin_amdgcn_ds_bpermute(i16 * 4, (int)asu(av[0][0])));
    v2u qf[5], kf[5], vf[5];
#pragma unroll
    for (int j = 0; j < 5; ++j) {
      f2 t0 = f2{aq[j][0], aq[j][1]} - m1[j] * cq[0], t1 = f2{aq[j][2], aq[j][3]} - m1[j] * cq[1];
      qf[j] = v2u{pk(t0.x, t0.y), pk(t1.x, t1.y)};
      t0 = (f2{ak[j][0], ak[j][1]} - m1[j] * ck[0]) * r1[j]; t1 = (f2{ak[j][2], ak[j][3]} - m1[j] * ck[1]) * r1[j];
      kf[j] = v2u{pk(t0.x, t0.y), pk(t1.x, t1.y)};
    }
    SCHED();
    const f4 z4 = {0.f, 0.f, 0.f, 0.f};
    const float big = (rt == 0 && g < 2) ? -1.0e30f : 0.f;
    f4 s[5], sp[5];
#pragma unroll
    for (int j = 0; j < 5; ++j) {
      s[j] = mma16(kf[j], qf[j], j == 0 ? bc + big : bc);
      if (j > 0) sp[j] = mma16(kf[j - 1], qf[j], bp);
      const f4 t = (av[j] - m4[j] * cv) * r4[j];
      vf[j] = v2u{pk(t[0], t[1]), pk(t[2], t[3])};
      SCHED();
    }
    float mx[5];
#pragma unroll
    for (int j = 0; j < 5; ++j) {
      if (j > 0) {
#pragma unroll
        for (int e = 0; e < 4; ++e) s[j][e] = fmx(s[j][e], sp[j][e]);
      }
      mx[j] = fmx(fmx(s[j][0], s[j][1]), fmx(s[j][2], s[j][3]));
      sp[j][0] = asf((u32)__builtin_amdgcn_ds_swizzle((int)asu(mx[j]), 0x401F));
      SCHED();
    }
    ;
#pragma unroll
    for (int j = 0; j < 5; ++j) {
      mx[j] = fmx(mx[j], sp[j][0]);
      sp[j][1] = asf((u32)__builtin_amdgcn_ds_bpermute((lane ^ 32) * 4, (int)asu(mx[j])));
      r1[j] *= 0.25f * 1.44269504f;
      SCHED();
    }
    const v2u ones = {0x3F803F80u, 0x3F803F80u};
    f4 sum[5], o[5];
#pragma unroll
    for (int j = 0; j < 5; ++j) {
      const float nm = -fmx(mx[j], sp[j][1]) * r1[j];
      float p[4];
#pragma unroll
      for (int e = 0; e < 4; ++e) p[e] = __builtin_amdgcn_exp2f(__builtin_fmaf(s[j][e], r1[j], nm));
      const u32 p0 = pk(p[0], p[1]), p1 = pk(p[2], p[3]);
      sum[j] = mma16(ones, v2u{p0, p1}, z4);
      if (j == 0) o[j] = mma16(vf[0], v2u{p0, p1}, z4);
      else o[j] = mma(v4u{vf[j - 1].x, vf[j - 1].y, vf[j].x, vf[j].y}, v4u{p0 & ~mc0, p1 & ~mc1, p0 & mc0, p1 & mc1}, z4);
      SCHED();
    }
#pragma unroll
    for (int j = 0; j < 5; ++j) {
      const f4 y = o[j] * __builtin_amdgcn_rcpf(sum[j][0]);
      *(LAS v2u*)(oa + j * 4096) = v2u{pk(y[0], y[1]), pk(y[2], y[3])};
    }
    if (!(DBG & 64)) STAMP(3);
    if (w < 4) {
      poll_flags(ws, WS_AFLAG + rep, lane, 2, tl, th);
      lwait(C_Q7, 8);
#pragma unroll
      for (int n = 11; n < 16; ++n) wo_issue((u32)(n * 4096));
#pragma unroll
      for (int n = 0; n < 6; ++n) w2a_issue((u32)(n * 4096));
    }
    BAR();
    STAMP(4);
    if (w < 4) {
#pragma unroll
      for (int j = 0; j < 3; ++j)
        if (j < 2 || w == 0) {
          const u32 a = L_OST + (u32)((w + 4 * j) * 2048 + lane * 16);
          v4u o0 = lds128(a), o1 = lds128(a + 1024);
          st128(o0, ws, vo[j]);
          st128(o1, ws, vo[j] + 1024);
        }
      if (w == 0) { WAITV(6); } else { WAITV(4); }
    }
    BAR();
    STAMP(7);
  } else {
    WAITV(0);
    BAR();
    STAMP(3);
    make_tag();
    if (w == 0) post(ws, WS_AFLAG, 252 + (xcd >> 1), lane, tl, th);
  }

  const int dh = w & 1, kh = (w >> 1) & 1;
  const bool hlp = w >= 4;
  const u32 vl = (u32)(lane * 16);
  f4 acc[4][2];
  u32 wa[4];
#pragma unroll
  for (int c = 0; c < 4; ++c) {
    wa[c] = wbase ^ (u32)((c << 5) ^ (dh << 6));
    acc[c][0] = acc[c][1] = f4{0.f, 0.f, 0.f, 0.f};
  }
  if (hlp) {
    v4u fa, fb;
    if (role == 2) {
#pragma unroll 1
      for (; t_w2 < 65536; t_w2 += 4096) { WAITV(PACE2); wo_issue(t_w2); WAITV(PACE2); w2a_issue(t_w2); }
      w2a_issue(65536);
    }
    asm volatile("buffer_load_dwordx4 %0, %2, %3, %4 offen sc1\n\tbuffer_load_dwordx4 %1, %2, %3, %4 offen offset:1024 sc1"
                 : "=&v"(fa), "=&v"(fb) : "v"(vl), "s"(ws), "s"(WS_AFLAG + rep));
    if (role == 0) {
#pragma unroll
      for (int n = 0; n < 12; ++n) { if (n >= 7) WAITV(PACE2); w2a_issue((u32)(n * 4096)); }
#pragma unroll
      for (int n = 0; n < 6; ++n) { WAITV(PACE2); wo_issue((u32)(n * 4096)); }
      t_w2 = 12 * 4096;
    } else {
#pragma unroll
      for (int n = 17; n < 23; ++n) w2a_issue((u32)(n * 4096));
      t_w2 = 23 * 4096;
    }
    asm volatile("s_waitcnt vmcnt(6)" : "+v"(fa), "+v"(fb)::"memory");
    if (!__all(tagged(fa, tl, th) && tagged(fb, tl, th))) poll_flags(ws, WS_AFLAG + rep, lane, 64, tl, th);
    lpost(C_A0);
    if (role == 0) STAMP(1);
    if (role == 0) {
#pragma unroll
      for (int n = 6; n < 16; ++n) { WAITV(PACE2); wo_issue((u32)(n * 4096)); }
    }
    STAMP(8);
    v4u of[8][2];
    const u32 so = WS_O + (u32)((4 * st + 2 * dh) * 16384 + (kh ^ vw) * 8192);
#pragma unroll
    for (int c = 0; c < 16; ++c)
      asm volatile("buffer_load_dwordx4 %0, %1, %2, %3 offen offset:%4 sc1" : "=v"(of[c & 7][c >> 3]) : "v"(vl), "s"(ws),
                   "s"(so + (u32)((c >> 3) * 16384 + ((c >> 2) & 1) * 4096)), "i"((c & 3) * 1024));
    WAITV(16);
    lpost(C_HB);
    if (role == 0) STAMP(2);
    if (role == 0) {
#pragma unroll
      for (int n = 12; n < 17; ++n) w2a_issue((u32)(n * 4096));
      t_w2 = 17 * 4096;
    }
    lwait(C_HB, 4);
    v4u wo[2][4];
    f4 ax[2][2];
    u32 wb[4];
#pragma unroll
    for (int d = 0; d < 4; ++d) { wb[d] = wa[d] + (u32)(kh * 32768); wo[0][d] = wfrag(wb[d]); ax[d >> 1][d & 1] = f4{0.f, 0.f, 0.f, 0.f}; }
    if (role == 0) { WAITV(5); } else { WAITV(0); }
    asm volatile(""
                 : "+v"(of[0][0]), "+v"(of[1][0]), "+v"(of[2][0]), "+v"(of[3][0]), "+v"(of[4][0]), "+v"(of[5][0]), "+v"(of[6][0]), "+v"(of[7][0]),
                   "+v"(of[0][1]), "+v"(of[1][1]), "+v"(of[2][1]), "+v"(of[3][1]), "+v"(of[4][1]), "+v"(of[5][1]), "+v"(of[6][1]), "+v"(of[7][1])::"memory");
    {
      u32 m = 0;
#pragma unroll
      for (int c = 0; c < 16; ++c) m = smax(of[c & 7][c >> 3], m);
      if (stale(m)) {
#pragma unroll 1
        for (; t_w2 < 23 * 4096; t_w2 += 4096) { WAITV(PACE2); w2h_issue(t_w2); }
#pragma unroll 1
        for (u32 n = 0; n < SPIN_CAP; ++n) {
          m = 0;
#pragma unroll
          for (int c = 0; c < 16; ++c) of[c & 7][c >> 3] = ld128(ws, vl + (c & 3) * 1024, so + (u32)((c >> 3) * 16384 + ((c >> 2) & 1) * 4096));
#pragma unroll
          for (int c = 0; c < 16; ++c) m = smax(of[c & 7][c >> 3], m);
          CNT(5);
          if (!stale(m)) break;
          asm volatile("s_sleep 8" ::: "memory");
        }
      }
    }
    if (b == 249 && w == 4 && lane == 0) st64(v2u{tl, th}, ws, WS_E);
    STAMP(9);
#pragma unroll
    for (int c = 0; c < 8; ++c) {
#pragma unroll
      for (int m = 0; m < 8; ++m) {
        const int d = m >> 1, a = m & 1;
        if (d < 2) acc[a][d] = mma(wo[c & 1][d], of[c][a], acc[a][d]);
        else ax[a][d - 2] = mma(wo[c & 1][d], of[c][a], ax[a][d - 2]);
        if (c + 1 < 8) whalf(wo[(c + 1) & 1][d], a, wb[d] + (u32)((c + 1) * 4096));
        if (m == 3 && c < 6 && t_w2 < 23 * 4096) { w2h_issue(t_w2); t_w2 += 4096; }
        SCHED();
      }
    }
    hbar(8);
    STAMP(10);
#pragma unroll
    for (int d = 0; d < 4; ++d) *(LAS f4*)(49152 + qtr * 4096 + d * 1024 + vl) = ax[d >> 1][d & 1];
#pragma unroll
    for (int n = 0; n < 12; ++n) {
      if (n == 3) { WAITV(3); lpost(C_A1); }
      w2b_issue((u32)(n * 4096));
    }
    lwait(C_A1, 4);
#pragma unroll
    for (int d = 0; d < 4; ++d) acc[2 + (d >> 1)][d & 1] = *(LAS f4*)(49152 + (qtr ^ 1) * 4096 + d * 1024 + vl);
    lpost(C_PRE);
    lwait(C_PRE, 8);
#pragma unroll
    for (int n = 12; n < 20; ++n) w2b_issue((u32)(n * 4096));
    STAMP(11);
  }
  {
    const u32 ub = WS_U + (u32)(((4 * T + 2 * dh) * 43 + (hlp ? fb0 : fa0) + kh) * 1024);
    const u32 uh = WS_U + (u32)(((4 * T + 2 * dh) * 43 + (hlp ? fb0 + 12 : fa1) + kh) * 1024);
    const u32 vl2 = vl + 43 * 1024;
#pragma unroll
    for (int c = 0; c < 4; ++c) wa[c] += (u32)((hlp ? 0 : 65536) + kh * 4096);
    v4u pre[2][4], uf[3][4];
    auto u_load = [&](int G, int k) { return ld128(ws, ((k & 1) ? vl2 : vl) + (k >> 1) * 2048, G < 3 ? ub + (u32)(G * 4096) : uh + (u32)((G - 3) * 4096)); };
    if (!hlp) {
      lwait(C_A0, 4);
#pragma unroll
      for (int n = 0; n < 2; ++n)
#pragma unroll
        for (int c = 0; c < 4; ++c) pre[n][c] = wfrag(wa[c] + (u32)(n * 8192));
      lpost(C_PRE);
      if (role != 0) lwait(C_HB, 8);
    }
#pragma unroll
    for (int n = 0; n < 12; ++n) { uf[n >> 2][n & 3] = u_load(n >> 2, n & 3); SCHED(); }
    if (hlp) {
      WAITV(20);
      lpost(C_B0);
      lwait(C_B0, 4);
#pragma unroll
      for (int n = 0; n < 2; ++n)
#pragma unroll
        for (int c = 0; c < 4; ++c) pre[n][c] = wfrag(wa[c] + (u32)(n * 8192));
    }
    STAMP(12);
    v4u xr;
    u32 b2r;
    const bool full = !hlp && kh == 0;
#pragma unroll
    for (int G = 0; G < 6; ++G) {
      if (G == 5 && hlp) break;
      v4u(&u)[4] = uf[G % 3];
      u32 m = smax(u[1], smax(u[0], 0)), m2 = smax(u[3], smax(u[2], 0));
      if (G < 5 || full) m = max(m, m2);
      if (stale(m)) {
#pragma unroll 1
        for (u32 n = 0; n < SPIN_CAP; ++n) {
          asm volatile("s_sleep 6" ::: "memory");
          if (hlp) { CNT(6); } else { CNT(11); }
#pragma unroll
          for (int k = 0; k < 4; ++k) u[k] = u_load(G, k);
          m = smax(u[1], smax(u[0], 0)); m2 = smax(u[3], smax(u[2], 0));
          if (G < 5 || full) m = max(m, m2);
          if (!stale(m)) break;
        }
        if (G == 0) {
#pragma unroll
          for (int k = 4; k < 12; ++k) uf[k >> 2][k & 3] = u_load(k >> 2, k & 3);
        }
      }
      if (G == 0 && hlp) asm volatile("s_mov_b64 exec, 1\n\tds_add_u32 %0, %1\n\ts_mov_b64 exec, -1" ::"v"(C_B1), "v"(1u) : "memory");
      if (G == 2) lwait(hlp ? C_B1 : C_A1, 4);
      if (G == 3) {
#pragma unroll
        for (int c = 0; c < 4; ++c) wa[c] += 65536;
      }
#pragma unroll
      for (int k = 0; k < 2; ++k) {
        const int n = 2 * G + k;
        if (n == 11 && !full) break;
#pragma unroll
        for (int c = 0; c < 4; ++c) {
          acc[c][0] = mma(u[2 * k], pre[k][c], acc[c][0]);
          SCHED();
          acc[c][1] = mma(u[2 * k + 1], pre[k][c], acc[c][1]);
          if (n + 2 < 12) pre[k][c] = wfrag(wa[c] + (u32)(((n + 2) & 7) * 8192));
          SCHED();
        }
        if (G < 2 || (G == 2 && !hlp)) { u[2 * k] = u_load(G + 3, 2 * k); u[2 * k + 1] = u_load(G + 3, 2 * k + 1); }
        if (n == 5) {
          xr = __builtin_amdgcn_raw_buffer_load_b128(rx, ov, 0, 0);
          b2r = ((const unsigned short*)pb2)[row];
        }
        SCHED();
      }
    }
    STAMP(13);
    if (!hlp) lpost(C_DONE);
    const float b2 = bflo(b2r);
    f4 lo = f4{bflo(xr[0]), bfhi(xr[0]), bflo(xr[1]), bfhi(xr[1])} + b2, hi = f4{bflo(xr[2]), bfhi(xr[2]), bflo(xr[3]), bfhi(xr[3])} + b2;
    const u32 rb = 81920u + (u32)((w >> 1) * 17408 + i16 * 272 + dh * 128 + g * 16);
    const u32 ra = 81920u + (u32)((tid >> 3) * 272 + (tid & 7) * 32);
    lwait(C_DONE, 4);
#pragma unroll
    for (int c = 0; c < 4; ++c) {
      const u32 a = rb + (u32)(((c ^ (2 * dh)) * 16) * 272);
      *(LAS f4*)a = acc[c][0];
      *(LAS f4*)(a + 64) = acc[c][1];
    }
    BAR();
#pragma unroll
    for (int p = 0; p < 4; ++p) { lo += *(LAS f4*)(ra + p * 17408); hi += *(LAS f4*)(ra + p * 17408 + 16); }
    *(v4u*)((char*)pout + ov) = v4u{pk(lo[0], lo[1]), pk(lo[2], lo[3]), pk(hi[0], hi[1]), pk(hi[2], hi[3])};
  }
  STAMP(14);
  asm volatile(".Lfk_end:" ::: "memory");
}

extern "C" void kernel_launch(void* const* d_in, const int* in_sizes, int n_in, void* d_out, int out_size, void* d_ws, size_t ws_size,
                              hipStream_t stream) {
  fused_kernel<<<256, 512, 0, stream>>>(d_in[0], d_in[1], d_in[2], d_in[4], d_in[5], d_in[6], d_in[7], d_out, d_ws);
}
```

```cpp
#include <hip/hip_runtime.h>
#pragma clang diagnostic ignored "-Wint-to-pointer-cast"

typedef unsigned u32;
typedef unsigned long long u64;
typedef u32 v4u __attribute__((ext_vector_type(4)));
typedef u32 v2u __attribute__((ext_vector_type(2)));
typedef float f4 __attribute__((ext_vector_type(4)));
typedef float f2 __attribute__((ext_vector_type(2)));
typedef short s4 __attribute__((ext_vector_type(4)));
typedef __bf16 bf8 __attribute__((ext_vector_type(8)));
typedef __bf16 bf2 __attribute__((ext_vector_type(2)));
typedef __amdgpu_buffer_rsrc_t rsrc_t;
#define LAS __attribute__((address_space(3)))
#define DI __device__ __forceinline__

constexpr int S = 2048, D = 512, F = 1376;
constexpr u32 WS_E = 0, WS_OFLAG = 4096, WS_HFLAG = 40960,

              WS_STATS = 81920, WS_H = 1u << 20, WS_O = 3u << 20, WS_U = 5u << 20, WS_R = 7u << 20, WS_END = 11u << 20;
constexpr u32 OOB = 0x80000000u, OOBS = 0x40000000u;
constexpr int SC1 = 16;
constexpr u32 LDS_BYTES = 163840;
constexpr u32 L_MISC = 39 * 4096;
constexpr u32 L_LN = 131072;
constexpr u32 C_WO = L_MISC, C_H = L_MISC + 4, C_GO = L_MISC + 8, C_W = L_MISC + 12  , C_A = L_MISC + 24, C_WR = L_MISC + 28,
              C_B0 = L_MISC + 32, C_PRE = L_MISC + 40, C_Q6 = L_MISC + 44, C_Q7 = L_MISC + 48, C_LN = L_MISC + 44  ,
              C_DONE = L_MISC + 52, C_OK = L_MISC + 56, C_P = L_MISC + 60, C_G = L_MISC + 64  , C_X = L_MISC + 96, C_O = L_MISC + 100, C_E = L_MISC + 104, C_D1 = L_MISC + 108, C_YB = L_MISC + 112,
              L_E = L_MISC + 512, L_B1 = L_MISC + 2048;
constexpr u32 SENT = 0xFFFFFFFFu;
#define NB1 10
#define URAMP 2
#define PDELAY 6
#define UW 4
#define PACE3 4
#define PACE 5
#define HOLD 12
#define PACE0 3
#define PACE2 6
constexpr u32 Q_STAGE = 45056, Q_X = 24576;
constexpr u32 L_QST = L_MISC + 2048;
static_assert(PACE0 <= PACE && PACE < 8 && PACE0 < HOLD, "map");
constexpr u32 SPIN_CAP = 1u << 18;
#define SCHED() __builtin_amdgcn_sched_barrier(0)

DI rsrc_t mk(const void* p, u32 n) { return __builtin_amdgcn_make_buffer_rsrc((void*)p, (short)0, (int)n, 0x00020000); }
template <int LOFF, int GOFF> DI void glds(rsrc_t rs, u32 voff, u32 soff, u32 lds) {
  asm volatile("s_add_u32 m0, %3, %4\n\ts_nop 0\n\tbuffer_load_dwordx4 %0, %1, %2 offen offset:%5 lds"
               ::"v"(voff), "s"(rs), "s"(soff), "s"(lds), "i"(LOFF - GOFF), "i"(GOFF) : "memory", "scc");
}
DI void gm0(u32 lds, int loff) { asm volatile("s_add_u32 m0, %0, %1" ::"s"(lds), "i"(loff) : "scc"); }
DI void gld(rsrc_t rs, u32 voff, u32 soff, int goff) {
  asm volatile("buffer_load_dwordx4 %0, %1, %2 offen offset:%3 lds" ::"v"(voff), "s"(rs), "s"(soff), "i"(goff) : "memory");
}
DI void gldc(rsrc_t rs, u32 voff, u32 soff, u32 lds, int loff) {
  asm volatile("s_add_u32 m0, %3, %4\n\ts_nop 0\n\tbuffer_load_dwordx4 %0, %1, %2 offen lds"
               ::"v"(voff), "s"(rs), "s"(soff), "s"(lds), "i"(loff) : "memory", "scc");
}
#define WAITV_(n) asm volatile("s_waitcnt vmcnt(" #n ")" ::: "memory")
#define WAITV(n) WAITV_(n)
#define BAR() asm volatile("s_waitcnt lgkmcnt(0)\n\ts_barrier" ::: "memory")
#define BAR0() asm volatile("s_barrier" ::: "memory")
#define FENCE() asm volatile("" ::: "memory")
#define NAP() asm volatile("s_sleep 4" ::: "memory")
DI v4u ld128(rsrc_t rs, u32 voff, u32 soff) { return __builtin_amdgcn_raw_buffer_load_b128(rs, voff, soff, SC1); }
DI v2u ld64(rsrc_t rs, u32 voff, u32 soff) { return __builtin_amdgcn_raw_buffer_load_b64(rs, voff, soff, SC1); }
DI void st128(v4u v, rsrc_t rs, u32 voff) { __builtin_amdgcn_raw_buffer_store_b128(v, rs, voff, 0, SC1); }
DI void st64(v2u v, rsrc_t rs, u32 voff) { __builtin_amdgcn_raw_buffer_store_b64(v, rs, voff, 0, SC1); }
DI float asf(u32 x) { return __builtin_bit_cast(float, x); }
DI u32 asu(float x) { return __builtin_bit_cast(u32, x); }
DI float bflo(u32 x) { return asf(x << 16); }
DI float bfhi(u32 x) { return asf(x & 0xffff0000u); }
DI u32 pk(float a, float b) { f2 p = {a, b}; bf2 q = __builtin_convertvector(p, bf2); return __builtin_bit_cast(u32, q); }
DI f4 mma(v4u a, v4u b, f4 c) {
  return __builtin_amdgcn_mfma_f32_16x16x32_bf16(__builtin_bit_cast(bf8, a), __builtin_bit_cast(bf8, b), c, 0, 0, 0);
}
DI f4 mma16(v2u a, v2u b, f4 c) {
  return __builtin_amdgcn_mfma_f32_16x16x16bf16_1k(__builtin_bit_cast(s4, a), __builtin_bit_cast(s4, b), c, 0, 0, 0);
}
DI v2u trd(u32 a) { return __builtin_bit_cast(v2u, __builtin_amdgcn_ds_read_tr16_b64_v4i16((LAS s4*)a)); }
DI void whalf(v4u& f, int h, u32 a) { v2u t = trd(a + 2048 * h); f[2 * h] = t.x; f[2 * h + 1] = t.y; }
DI v4u wfrag(u32 a) { v2u lo = trd(a), hi = trd(a + 2048); return v4u{lo.x, lo.y, hi.x, hi.y}; }
DI v4u lds128(u32 a) { return *(LAS v4u*)a; }
template <int CTRL> DI float dpp(float v) { return asf((u32)__builtin_amdgcn_update_dpp(0, (int)asu(v), CTRL, 0xf, 0xf, true)); }
DI float fmx(float a, float b) { return __builtin_elementwise_maximum(a, b); }
DI float row_sum(float v) {
  v += dpp<0xB1>(v);
  v += dpp<0x4E>(v);
  v += dpp<0x141>(v);
  v += dpp<0x140>(v);
  return v;
}
DI u32 umax3(u32 a, u32 b, u32 c) { return max(max(a, b), c); }
DI u32 smax(v4u v, u32 m) { return umax3(v.x, v.z, m); }
DI bool stale(u32 m) { return __any(m == SENT); }
DI bool tagged(v4u f, u32 tl, u32 th) { return f.x == tl && f.y == th && f.z == tl && f.w == th; }
DI void poll2(rsrc_t ws, u32 voff, u32 soff, u32 tl, u32 th) {
#pragma unroll 1
  for (u32 n = 0; n < SPIN_CAP; ++n) {
    if (__all(tagged(ld128(ws, voff, soff), tl, th))) break;
    NAP();
  }
  FENCE();
}
DI void poll1(rsrc_t ws, u32 voff, u32 soff, u32 tl, u32 th) {
#pragma unroll 1
  for (u32 n = 0; n < SPIN_CAP; ++n) {
    const v2u f = ld64(ws, voff, soff);
    if (__all(f.x == tl && f.y == th)) break;
    asm volatile("s_sleep 10" ::: "memory");
  }
  FENCE();
}
DI void post(rsrc_t ws, u32 off, int lane, u32 tl, u32 th) {
  if (lane < 8) st64(v2u{tl, th}, ws, off + (u32)(lane * 4096));
}
DI u32 arrive(u32 addr) {
  u32 old;
  asm volatile("s_mov_b64 exec, 1\n\tds_add_rtn_u32 %0, %1, %2\n\ts_mov_b64 exec, -1\n\ts_waitcnt lgkmcnt(0)" : "=&v"(old) : "v"(addr), "v"(1u) : "memory");
  return __builtin_amdgcn_readfirstlane(old);
}
DI void lpost(u32 addr) {
  asm volatile("s_waitcnt lgkmcnt(0)\n\ts_mov_b64 exec, 1\n\tds_add_u32 %0, %1\n\ts_mov_b64 exec, -1" ::"v"(addr), "v"(1u) : "memory");
}
DI void lpost0(u32 addr) {
  asm volatile("s_mov_b64 exec, 1\n\tds_add_u32 %0, %1\n\ts_mov_b64 exec, -1" ::"v"(addr), "v"(1u) : "memory");
}
DI u32 lpeek(u32 addr) { return __hip_atomic_load((LAS u32*)addr, __ATOMIC_RELAXED, __HIP_MEMORY_SCOPE_WORKGROUP); }
template <int SLEEP = 0> DI void lwait(u32 addr, u32 target) {
  if (__builtin_expect(__builtin_amdgcn_readfirstlane(lpeek(addr)) < target, 0)) {
#pragma unroll 1
    for (u32 n = 0; n < (1u << 24); ++n) {
      if (SLEEP) asm volatile("s_sleep %0" ::"i"(SLEEP) : "memory");
      else asm volatile("s_nop 3" ::: "memory");
      if (__builtin_amdgcn_readfirstlane(lpeek(addr)) >= target) break;
    }
  }
  FENCE();
}
DI void hbar(u32 addr) { lpost(addr); lwait(addr, 4); }

template <int NB, class SEND> DI void u_loop(f4 (&acc)[3][4], rsrc_t ws, u32 vh, u32 hb, u32 wbase, int rot, SEND send) {
  v4u hf[8][NB], wf[2][4];
  asm volatile("; blocks %0" ::"i"(NB) : "memory");
  u32 sc = (u32)(rot * 32768);
  vh += hb;
#pragma unroll
  for (int k = 0; k < 7; ++k) {
#pragma unroll
    for (int j = 0; j < NB; ++j) { hf[k][j] = ld128(ws, vh + j * 256, sc); SCHED(); }
    sc = (sc + 32768) & 0xFFFFF;
  }
  u32 wa[4];
#pragma unroll
  for (int c = 0; c < 4; ++c) { wa[c] = wbase ^ (u32)(c << 5); wf[0][c] = wfrag(wa[c]); }
  SCHED();
  u32 cw = C_W, cg = C_G;
#pragma unroll 1
  for (int n = 1; n < 4; ++n) {
    u32 seen;
#pragma unroll
    for (int k = 0; k < 8; ++k) {
      if (k == 7 && __builtin_expect(__builtin_amdgcn_readfirstlane(seen) < 4, 0)) lwait(cw, 4);
#pragma unroll
      for (int m = 0; m < 4 * NB; ++m) {
        const int j = NB - 1 - m % NB, c = (m / NB) ^ 1;
        acc[j][c] = mma(wf[k & 1][c], hf[k][j], acc[j][c]);
        if (m < 8) whalf(wf[(k + 1) & 1][m >> 1], m & 1, wa[m >> 1] + (u32)((k + 1) * 4096));
        if (k == 7 && m >= 4 * NB - 4) wa[m & 3] += 32768;
        if (k == 5 && m == 0) seen = lpeek(cw);
        if (k == 7 && m == 0) lpost0(cg);
        if (m % 3 == 2 && m / 3 < NB)
          hf[(k + 7) & 7][m / 3] = ld128(ws, vh + (m / 3) * 256, sc);
        if (m == 4 * NB - 1) sc = (sc + 32768) & 0xFFFFF;
        SCHED();
      }
    }
    cw += 4; cg += 4;
  }
#pragma unroll
  for (int j = 0; j < NB; ++j) hf[7][j] = ld128(ws, vh + j * 256, sc);
  v4u wg[3][2];
  wg[0][0] = wf[0][2]; wg[0][1] = wf[0][3];
  wg[1][0] = wfrag(wa[2] + 4096); wg[1][1] = wfrag(wa[3] + 4096);
  SCHED();
#pragma unroll
  for (int q = 0; q < 16; ++q) {
    const int k = q & 7, cb = q < 8 ? 2 : 0, q2 = q + 2;
#pragma unroll
    for (int m = 0; m < 2 * NB; ++m) {
      const int j = m % NB, c = m / NB;
      acc[j][cb + c] = mma(wg[q % 3][c], hf[k][j], acc[j][cb + c]);
      if (m < 4 && q2 < 16) whalf(wg[q2 % 3][m >> 1], m & 1, wa[(q2 < 8 ? 2 : 0) + (m >> 1)] + (u32)((q2 & 7) * 4096));
      if (q == 14 && m == 0) lpost0(cg);
      if (q >= 9 && q < 9 + 2 * NB && m == 1) send(q - 9);
      SCHED();
    }
  }
}

__global__ __launch_bounds__(512, 2) void fused_kernel(const void* __restrict__ px, const void* __restrict__ pwqkv, const void* __restrict__ pwo,
                                                     const void* __restrict__ pw1, const void* __restrict__ pb1, const void* __restrict__ pw2,
                                                     const void* __restrict__ pb2, void* __restrict__ pout, void* __restrict__ pws) {
  __shared__ __attribute__((aligned(1024))) char lds_mem[LDS_BYTES];
  asm volatile("" ::"v"((u32)(size_t)(LAS char*)lds_mem));
  const int tid = threadIdx.x, lane = tid & 63;
  const int b = blockIdx.x, xcd = b & 7, r = b >> 3;
  {
    u64 pc;
    u32 len;
    asm volatile("s_getpc_b64 %0\n.Lfk_pc:\n\ts_mov_b32 %1, .Lfk_end-.Lfk_pc" : "=s"(pc), "=s"(len));
    const rsrc_t rc = mk((const void*)pc, len);
    const u32 vo = (tid & 31) == 0 ? (u32)(((tid >> 5) * 32 + r) * 128) : OOB;
    asm volatile("s_mov_b32 m0, %2\n\ts_nop 0\n\tbuffer_load_dword %0, %1, 0 offen lds" ::"v"(vo), "s"(rc), "s"(L_MISC + 1024) : "memory");
  }
  const int w = __builtin_amdgcn_readfirstlane(tid >> 6);
  const int g = lane >> 4, i16 = lane & 15;
  const int odd = xcd & 1, nU = odd ? 15 : 18;
  int role = r < nU ? 0 : (r < 31 || !odd) ? 1 : 2;
  asm volatile("" ::"s"(px), "s"(pwqkv), "s"(pwo), "s"(pw1), "s"(pb1), "s"(pw2), "s"(pb2), "s"(pout), "s"(pws));
  const rsrc_t rx = mk(px, S * D * 2);
  const int kb = (xcd & 2) * 33 + odd * 36 + (xcd >> 2) * nU + r;
  if (role == 0 && w < 4) {
    const u32 sx = (u32)((kb * 16 + 8 * (w & 1) + (w >> 1)) * 1024), lx = L_LN + (u32)(((w >> 1) * 8 + (w & 1) * 4) * 1088);
    glds<0, 0>(rx, (u32)(lane * 16), sx, lx);
    glds<1088, 2048>(rx, (u32)(lane * 16), sx, lx);
    glds<2 * 1088, 0>(rx, (u32)(lane * 16), sx + 4096, lx);
    glds<3 * 1088, 2048>(rx, (u32)(lane * 16), sx + 4096, lx);
  }
  SCHED();
  const int qi = (xcd & 2) * 15 + odd * 17 + r - 18;
  const int rt = qi >> 2, hgl = qi & 3, hg = (xcd & 4) + hgl;
  const int qtr = w & 3;
  const int rho = ((tid >> 3) & 31);
  const u32 csrc = (u32)(((lane & 7) ^ (rho & 6)) * 16);
  const int knat = ((rho >> 2) & 3) * 8 + (rho >> 4) * 4 + (rho & 3);
  const rsrc_t rwq = mk(pwqkv, D * 1536 * 2);
  u32 v_wq = 0, v_x[3] = {0, 0, 0};
  const int c0 = (2 * xcd + (xcd & 4) + ((hgl + rt) & 1)) & 7;
  const u32 wl = (u32)(w * 1024);
#define Q_PIECE(C, K)                                                                                      \
  do {                                                                                                     \
    const int ch_ = ((C) + c0) & 7;                                                                        \
    constexpr int sb_ = ((C) % 3) * Q_STAGE, j_ = (K) >> 1;                                                \
    if (((K) & 1) == 0) glds<sb_ + j_ * 8192, j_ * 1024>(rwq, v_wq, (u32)(ch_ * 64 * 3072), wl);           \
    else if (j_ < 2 || w < 4) glds<sb_ + Q_X + j_ * 8192, 0>(rx, v_x[j_], (u32)(ch_ * 128), wl);           \
  } while (0)
#define Q_PRE(C, K) gm0(wl, ((C) % 3) * Q_STAGE + (((K) & 1) ? Q_X + ((K) >> 1) * 8192 : ((K) >> 1) * 7168))
#define Q_POST(C, K)                                                                                       \
  do {                                                                                                     \
    const int ch_ = ((C) + c0) & 7;                                                                        \
    if (((K) & 1) == 0) gld(rwq, v_wq, (u32)(ch_ * 64 * 3072), ((K) >> 1) * 1024);                         \
    else if ((K) < 5 || w < 4) gld(rx, v_x[(K) >> 1], (u32)(ch_ * 128), 0);                                \
  } while (0)
  if (role == 1) {
    v_wq = (u32)(((tid >> 8) * 32 + knat) * 3072 + hg * 128) + csrc;
    Q_PIECE(0, 0); Q_PIECE(0, 2); Q_PIECE(0, 4);
#pragma unroll
    for (int j = 0; j < 3; ++j) {
      const int R = (tid >> 3) + 64 * j;
      const int row = 144 * rt - 16 + 2 * R - (R >= 80 ? 159 : 0);
      v_x[j] = (u32)row < (u32)S ? (u32)(row * 1024 + (((lane & 7) ^ ((R >> 1) & 7)) * 16)) : OOB;
    }
    Q_PIECE(0, 1); Q_PIECE(0, 3); Q_PIECE(0, 5);
  }
  SCHED();
  const rsrc_t ws = mk(pws, WS_END);
  const int cl = (r * 11) >> 5, dt = r - 3 * cl;
  const int col = (xcd >> 1) * 11 + odd * 6 + cl;
  const int rot = (dt * 11 + 8 * xcd) & 31;
  if (w == 7 && role != 0)
    asm volatile("s_mov_b32 m0, %2\n\ts_nop 0\n\tbuffer_load_dword %0, %1, 0 offen sc1 lds" ::"v"(lane < 2 ? (u32)(lane * 4) : OOB), "s"(ws), "s"(L_MISC + 512) : "memory");
  if (tid < 8) *(LAS v4u*)(L_MISC + tid * 16) = v4u{0u, 0u, 0u, 0u};
  asm volatile("" : "+s"(role));
  SCHED();

  u32 tl = 0, th = 0;
  bool last = false;
  auto make_tag = [&]() __attribute__((always_inline)) {
    const v2u ev = *(LAS v2u*)L_E;
    u64 e = ((u64)__builtin_amdgcn_readfirstlane(ev.y) << 32) | (u32)__builtin_amdgcn_readfirstlane(ev.x);
    e = e * 6364136223846793005ull + 1442695040888963407ull;
    tl = (u32)e;
    th = (u32)(e >> 32);
  };
  if (role == 0 && w < 4) {
    BAR();
    u32 ha = L_LN + (u32)(((w >> 1) * 8 + (w & 1) * 4 + g) * 1088 + i16 * 16);
    u32 vst = (kb < 128 && i16 == 0) ? WS_STATS + (u32)((w >> 1) * 8192 + kb * 64 + (w & 1) * 32 + g * 8) : OOB;
    u32 ta = L_LN + (u32)(((w >> 1) * 8 + (i16 >> 2)) * 1088 + (w & 1) * 512 + g * 32 + (i16 & 3) * 8);
    u32 vo = kb < 128 ? WS_H + (u32)(((w >> 1) * 128 + kb) * 8192 + (w & 1) * 4096 + lane * 16) : OOB;
    asm volatile("s_waitcnt vmcnt(0)" : "+v"(ha), "+v"(vst), "+v"(ta), "+v"(vo)::"memory");
    v4u xv[4];
#pragma unroll
    for (int j = 0; j < 4; ++j) xv[j] = lds128(ha + j * 256);
    f2 a[16];
#pragma unroll
    for (int j = 0; j < 16; ++j) a[j] = f2{bflo(xv[j >> 2][j & 3]), bfhi(xv[j >> 2][j & 3])};
    f2 t0 = a[0] + a[1], t1 = a[2] + a[3];
#pragma unroll
    for (int j = 4; j < 16; j += 2) { t0 += a[j]; t1 += a[j + 1]; }
    t0 += t1;
    const float m = row_sum(t0.x + t0.y) * (1.f / D);
#pragma unroll
    for (int j = 0; j < 16; ++j) a[j] -= m;
    t0 = a[0] * a[0]; t1 = a[1] * a[1];
#pragma unroll
    for (int j = 2; j < 16; j += 2) { t0 += a[j] * a[j]; t1 += a[j + 1] * a[j + 1]; }
    t0 += t1;
    const float var = row_sum(t0.x + t0.y) * (1.f / D);
    const float rs = __builtin_amdgcn_rsqf(var + 1e-5f);
    const float sc = rs * __builtin_amdgcn_rsqf(var * rs * rs + 1e-5f);
#pragma unroll
    for (int j = 0; j < 4; ++j) {
      v4u o;
#pragma unroll
      for (int e = 0; e < 4; ++e) { const f2 y = a[4 * j + e] * sc; o[e] = pk(y.x, y.y); }
      *(LAS v4u*)(ha + j * 256) = o;
    }
    lpost0(C_LN + (w >> 1) * 4);
    st64(v2u{asu(m), asu(rs)}, ws, vst);
    lwait(C_LN + (w >> 1) * 4, 2);
#pragma unroll
    for (int j = 0; j < 4; ++j) {
      v2u lo = trd(ta + j * 128), hi = trd(ta + j * 128 + 4 * 1088);
      st128(v4u{lo.x, lo.y, hi.x, hi.y}, ws, vo + j * 1024);
    }
    SCHED();
    lwait(C_E, 1);
    make_tag();
    u32 vfl = lane < 8 ? WS_HFLAG + (u32)((kb < 128 ? kb * 8 : kb * 8 + 32) + lane * 4096) : OOB;
    v2u tv = {tl, th};
    asm volatile("s_waitcnt vmcnt(0)" : "+v"(vfl), "+v"(tv)::"memory");
    last = arrive(C_H) == 3;
    if (last) st64(tv, ws, vfl);
    SCHED();
  }
  const u32 wbase = (u32)((lane >> 2) * 128 + ((lane >> 3) & 3) * 32 + (lane & 3) * 8);
  const int st = 4 * xcd + (r & 3), T = ((r >> 2) + 2 * r) & 7;
  const int vy = T & 1, vz = (T >> 1) & 1, vw = (r >> 3) & 1;
  const rsrc_t rw2 = mk(pw2, F * S * 2), rwo = mk(pwo, D * D * 2);
  const u32 v_w2 = (u32)(rho * 4096 + st * 128) + csrc, v_wo = (u32)(knat * 1024 + T * 128) + csrc;
  const u32 ql = (u32)(qtr * 1024);
  const int fa0 = (vy ? 20 : 0) + (vz ? 11 : 0), fa1 = (vy ? 20 : 0) + (vz ? 0 : 12), fb0 = vy ? 0 : 23;
  const u32 v_w2a = v_w2 + (u32)(fa0 * 131072), v_w2h = v_w2 + (u32)(fa1 * 131072), v_w2b = v_w2 + (u32)(fb0 * 131072);
  const u32 v_wl = v_wo + (u32)(vw << 18), v_wh = v_wo + (u32)((vw ^ 1) << 18);
  auto wo_issue = [&](u32 t) __attribute__((always_inline)) { gldc(rwo, t < 32768 ? v_wl : v_wh, (t & 32767) << 3, ql, (int)t); };
  auto w2a_issue = [&](u32 t) __attribute__((always_inline)) { gldc(rw2, t < 49152 ? v_w2a : v_w2h, (t < 49152 ? t : t - 49152) << 5, ql, (int)t + 65536); };
  auto w2b_issue = [&](u32 t) __attribute__((always_inline)) { gldc(rw2, v_w2b, t << 5, ql, (int)t); };
  const u32 rep = (u32)(xcd * 4096);

  if (role == 0) {
    const int par = col >= 22, line = col - par;
    int fkeep = 2 * line, fsend = 2 * line + 1;
    if (fkeep >= 43) fkeep -= 43;
    if (fsend >= 43) fsend -= 43;
    const bool keep = col != 22, send = col != 21;
    const int lb0 = (dt == 2 && qtr == 3) ? 8 : 3 * qtr;
    const bool three = qtr < 2 || (qtr == 2 && dt < 2);
    const u32 vr = (u32)(lb0 * 2048 + lane * 16);
    const u32 vs = send ? WS_R + (u32)((dt * 43 + fsend) * 24576) + vr : OOB;
    const u32 vu = keep ? WS_U + (u32)(((dt * 11 + lb0) * 43 + fkeep) * 1024 + lane * 16) : OOB;
    if (w < 4) {
      v4u tg = {tl & 0xFFFu, (tl >> 12) & 0xFFFu, th & 0xFFFu, (th >> 12) & 0xFFFu};
      u32 vh = (u32)(g * 8192 + ((dt * 11 + lb0) * 16 + i16) * 16);
      const u32 hb = WS_H + (u32)(par * 128 * 8192);
      f4 acc[3][4];
      {
        const v2u b0 = *(LAS v2u*)(L_B1 + g * 8), b1 = *(LAS v2u*)(L_B1 + g * 8 + 32);
#pragma unroll
        for (int j = 0; j < 3; ++j) {
          acc[j][0] = f4{bflo(b0.x), bfhi(b0.x), bflo(b0.y), bfhi(b0.y)};
          acc[j][1] = f4{bflo(b1.x), bfhi(b1.x), bflo(b1.y), bfhi(b1.y)};
          acc[j][2] = acc[j][3] = f4{0.f, 0.f, 0.f, 0.f};
          asm volatile("" : "+v"(acc[j][0]), "+v"(acc[j][1]), "+v"(acc[j][2]), "+v"(acc[j][3]));
        }
        asm volatile("" : "+v"(tg), "+v"(vh));
      }
      if (last) {
        WAITV(0);
        if (PDELAY) asm volatile("s_sleep %0" ::"i"(PDELAY) : "memory");
        poll2(ws, (u32)(lane * 16), WS_HFLAG + rep, tl, th);
        lpost(C_GO);
      }
      lwait(C_GO, 5);
      auto sendp = [&](int p) __attribute__((always_inline)) { st128((__builtin_bit_cast(v4u, acc[p >> 1][2 + (p & 1)]) & ~0xFFFu) | tg, ws, vs + p * 1024); };
      if (three) u_loop<3>(acc, ws, vh, hb, wbase, rot, sendp);
      else u_loop<2>(acc, ws, vh, hb, wbase, rot, sendp);
      if (keep) {
        WAITV(0);
        v4u pr[3][2];
        const u32 v2 = three ? vr + 4096 : OOB;
#pragma unroll 1
        for (u32 n = 0; n < SPIN_CAP; ++n) {
          u32 m = 0;
#pragma unroll
          for (int j = 0; j < 3; ++j) {
            pr[j][0] = ld128(ws, j < 2 ? vr + j * 2048 : v2, WS_R + (u32)((dt * 43 + fkeep) * 24576));
            pr[j][1] = ld128(ws, (j < 2 ? vr + j * 2048 : v2) + 1024, WS_R + (u32)((dt * 43 + fkeep) * 24576));
          }
#pragma unroll
          for (int j = 0; j < 3; ++j) {
            pr[j][0] ^= tg; pr[j][1] ^= tg;
            const v4u d = pr[j][0] | pr[j][1];
            const u32 e = d.x | d.y | d.z | d.w;
            m |= (j < 2 || three) ? e : 0u;
          }
          if (!__any((m & 0xFFFu) != 0)) break;
          NAP();
        }
        lwait(C_OK, 1);
#pragma unroll
        for (int j = 0; j < 3; ++j)
          if (j < 2 || three) {
            v4u o;
#pragma unroll
            for (int h = 0; h < 4; ++h) {
              const int t = h >> 1, e = (h & 1) * 2;
              const f2 a = f2{acc[j][t][e], acc[j][t][e + 1]} + f2{asf(pr[j][t][e]), asf(pr[j][t][e + 1])};
              const f2 z = a * (a * a * -0.1029432397f + -2.302208198f);
              const f2 d = f2{__builtin_amdgcn_exp2f(z.x), __builtin_amdgcn_exp2f(z.y)} + 1.f;
              const f2 y = a * f2{__builtin_amdgcn_rcpf(d.x), __builtin_amdgcn_rcpf(d.y)};
              o[h] = pk(y.x, y.y);
            }
            o.x = min(o.x, SENT - 1); o.z = min(o.z, SENT - 1);
            st128(o, ws, vu + j * 43 * 1024);
          }
      }
    } else {
      const rsrc_t rw1 = mk(pw1, S * F * 2), rb1 = mk(pb1, F * 2);
      const u32 v_w1 = (u32)(knat * 5504 + line * 128) + csrc;
      u32 t1 = (u32)rot;
      BAR();
      if (w == 7)
        asm volatile("s_mov_b32 m0, %2\n\ts_nop 0\n\tbuffer_load_dword %0, %1, 0 offen sc1 lds" ::"v"(lane < 2 ? (u32)(lane * 4) : OOB), "s"(ws), "s"(L_E) : "memory");
      if (w == 7)
        asm volatile("s_mov_b32 m0, %3\n\ts_nop 0\n\tbuffer_load_dword %0, %1, %2 offen lds" ::"v"(lane < 16 ? (u32)(lane * 4) : OOB), "s"(rb1), "s"((u32)(fkeep * 64)), "s"(L_B1) : "memory");
#pragma unroll
      for (int n = 0; n < 32; ++n) {
        if (n == HOLD) lwait<1>(C_H, 4);
        if (n < HOLD) { WAITV(PACE0); } else { WAITV(PACE); }
        if (n == PACE0 && w == 7) lpost(C_E);
        if (n == 8 + PACE) lpost(C_GO);
        if (n == 16 + PACE) { lpost(C_W); }
        if (n == 24 + PACE) lpost(C_W + 4);
        t1 &= 31;
        gldc(rw1, v_w1, t1 * (32 * 5504), ql, n * 4096);
        t1 += 1;
      }
      lwait(C_E, 1);
      make_tag();
      WAITV(0);
      lpost(C_W + 8);
    }
  } else if (role == 1) {
    Q_PIECE(1, 0); Q_PIECE(1, 1); Q_PIECE(1, 2); Q_PIECE(1, 3); Q_PIECE(1, 4); Q_PIECE(1, 5);
    const int hd = w & 3, par = w >> 2;
    u32 wa[2], xa[2][2];
    wa[0] = wbase ^ (u32)(hd << 5);
    xa[0][0] = Q_X + (u32)(par * 10240 + i16 * 128 + ((g ^ (i16 >> 1)) << 4));
    xa[0][1] = xa[0][0] ^ 64u;
    wa[1] = wa[0] + 65536; xa[1][0] = xa[0][0] + 65536; xa[1][1] = xa[0][1] + 65536;
    asm volatile("" : "+v"(wa[1]), "+v"(xa[1][0]), "+v"(xa[1][1]));
    f4 aq[5], ak[5], av[5];
#pragma unroll
    for (int j = 0; j < 5; ++j) aq[j] = ak[j] = av[j] = f4{0.f, 0.f, 0.f, 0.f};
    v4u xf[2][5], wf[2][3];
    auto q_read1 = [&](int set, int c, int step, int m) __attribute__((always_inline)) {
      if (m == 0 || m >= 7) {
        const int j = m == 0 ? 4 : 10 - m, o = (c % 3) * Q_STAGE + j * 2048;
        xf[set][j] = lds128(o < 65536 ? xa[0][step] + (u32)o : xa[1][step] + (u32)(o - 65536));
      } else {
        const int k = (m - 1) >> 1, h = (m - 1) & 1, o = (c % 3) * Q_STAGE + k * 8192 + step * 4096;
        whalf(wf[set][k], h, o + 2048 < 65536 ? wa[0] + (u32)o : wa[1] + (u32)(o - 65536));
      }
    };
#define LGKM(n) __builtin_amdgcn_s_waitcnt(0xC07F | ((n) << 8))
#define Q_STEP(SET, RC, RSTP, CI, K0, HOOK)                                                               \
  _Pragma("unroll") for (int m = 0; m < 15; ++m) {                                                         \
    const int j = 4 - m / 3;                                                                               \
            \
    const int pc = (HOOK) == 9 ? (m == 1 ? 0 : m == 3 ? 1 : m == 5 ? 2 : m == 11 ? 3 : m == 12 ? 4 : m == 13 ? 5 : -1) \
                               : (CI) < 7 ? (m == 3 ? (K0) : m == 11 ? (K0) + 1 : m == 13 ? (K0) + 2 : -1) : -1;       \
    if ((SET) == 0 && m == 0) LGKM(4);                                                                     \
    if ((SET) == 0 && m == 3) LGKM(3);                                                                     \
    if (pc >= 0) { Q_PRE((CI) & 7, pc); SCHED(); }                                                         \
           \
    if (m >= 6 && m < 10) xf[SET][0][m - 6] = i16 == 0 ? 0x3F803F80u : xf[SET][0][m - 6];                  \
    if (m % 3 == 0) aq[j] = mma(wf[SET][0], xf[SET][j], aq[j]);                                            \
    else if (m % 3 == 1) ak[j] = mma(wf[SET][1], xf[SET][j], ak[j]);                                       \
    else av[j] = mma(xf[SET][j], wf[SET][2], av[j]);                                                       \
    if (pc >= 0) { SCHED(); Q_POST((CI) & 7, pc); }                                                        \
    if ((RC) < 8 && m < 11) q_read1(1 - (SET), RC, RSTP, m);                                              \
    q_hook(HOOK, m);                                                                                       \
    SCHED();                                                                                               \
  }
    f4 bc, bp;
#pragma unroll
    for (int e = 0; e < 4; ++e) {
      const int dk = i16 - (4 * g + e);
      bc[e] = (u32)dk < 8u ? 0.f : -1.0e30f;
      bp[e] = dk <= -9 ? 0.f : -1.0e30f;
    }
    const int dk0 = i16 - 4 * g;
    u32 mc0 = (dk0 >= 0 ? 0xffffu : 0u) | (dk0 >= 1 ? 0xffff0000u : 0u), mc1 = (dk0 >= 2 ? 0xffffu : 0u) | (dk0 >= 3 ? 0xffff0000u : 0u);
    u32 vo[3];
#pragma unroll
    for (int P = 0; P < 3; ++P) {
      const int q = 4 * P + 2 * (g & 1) + (i16 >> 3) - 1, sb = 9 * rt + q;
      vo[P] = ((u32)q < 9u && sb < 128) ? WS_O + (u32)((sb * 16 + hg * 2 + (hd >> 1)) * 1024 + ((hd & 1) * 2 + (g >> 1)) * 256 + (par * 8 + (i16 & 7)) * 16) : OOB;
    }
    const u32 sta = L_QST + (u32)(par * 1024);
    u32 sw = sta + (u32)(lane * 4), sa1 = sta + (u32)(i16 * 4), sa4 = sta + (u32)(g * 16);
    const int p0 = 72 * rt - 8 + lane, p1 = p0 + 64;
    u32 vs0 = (u32)p0 < 1024u ? (u32)(p0 * 8) : OOB, vs1 = (lane < 16 && p1 < 1024) ? (u32)(p1 * 8) : OOB;
    asm volatile("" : "+v"(bc), "+v"(bp), "+v"(mc0), "+v"(mc1), "+v"(vo[0]), "+v"(vo[1]), "+v"(vo[2]), "+v"(sw), "+v"(sa1), "+v"(sa4), "+v"(vs0), "+v"(vs1));
    WAITV(5);
    BAR();
#pragma unroll
    for (int m = 0; m < 11; ++m) q_read1(0, 0, 0, m);
    Q_PIECE(2, 0); Q_PIECE(2, 1); Q_PIECE(2, 2);
    make_tag();
    v4u fa;
    v2u s0, s1;
    float m1[5], r1[5];
    f4 m4[5], r4[5];
    auto q_hook = [&](int hook, int m) __attribute__((always_inline)) {
      if (hook == 11 && m >= 6 && (m & 1) == 0 && w < 4) w2a_issue((u32)((6 + (m - 6) / 2) * 4096));
      if (hook == 12 && m < 12 && (m & 1) == 1 && w < 4) w2a_issue((u32)((11 + m / 2) * 4096));
      if (hook == 13 && m == 1 && w < 4) lwait(C_Q6, 8);
      if ((hook == 13 || hook == 14) && m % 3 == 2 && w < 4) wo_issue((u32)(((hook - 13) * 5 + m / 3) * 4096));
      if (hook == 15 && m == 2 && w < 4) wo_issue(10 * 4096);
      if (hook == 10 && m == 0) asm volatile("buffer_load_dwordx4 %0, %1, %2, %3 offen sc1" : "=v"(fa) : "v"((u32)(lane * 16)), "s"(ws), "s"(WS_HFLAG + rep));
      if (hook == 11 && m == 1) {
        if (__builtin_expect(!__all(tagged(fa, tl, th)), 0)) poll2(ws, (u32)(lane * 16), WS_HFLAG + rep, tl, th);
        FENCE();
      }
      if (hook == 11 && m == 3) asm volatile("buffer_load_dwordx2 %0, %1, %2, %3 offen sc1" : "=v"(s0) : "v"(vs0), "s"(ws), "s"(WS_STATS + (u32)(par * 8192)));
      if (hook == 11 && m == 5) asm volatile("buffer_load_dwordx2 %0, %1, %2, %3 offen sc1" : "=v"(s1) : "v"(vs1), "s"(ws), "s"(WS_STATS + (u32)(par * 8192)));
      if (hook == 13 && m == 11) { *(LAS u32*)sw = s0.x; *(LAS u32*)(sw + 512) = s0.y; }
      if (hook == 13 && m == 12) { *(LAS u32*)(sw + 256) = s1.x; *(LAS u32*)(sw + 768) = s1.y; }
      if (hook == 15) {
        if (m < 5) { m1[m] = *(LAS float*)(sa1 + m * 64); r1[m] = *(LAS float*)(sa1 + 512 + m * 64); }
        else if (m < 10) { m4[m - 5] = *(LAS f4*)(sa4 + (m - 5) * 64); r4[m - 5] = *(LAS f4*)(sa4 + 512 + (m - 5) * 64); }
      }
    };
#define Q_CHUNK(C)                                                                                         \
  Q_STEP(0, C, 1, (C) + 2, 3, 2 * (C))                                                                     \
  if ((C) < 5) { WAITV(5); LGKM(0); BAR0(); }                                                                        \
  else if ((C) == 5) { asm volatile("s_waitcnt vmcnt(0)" : "+v"(fa)::"memory"); LGKM(0); BAR0(); if (w >= 4) __builtin_amdgcn_s_setprio(1); } \
  else if ((C) == 6) { if (w >= 4) { WAITV(0); } else { WAITV(11); } asm volatile("" : "+v"(s0), "+v"(s1)); LGKM(0); lpost0(C_Q6); } \
  else { LGKM(0); lpost0(C_Q7); }                                                                                 \
  SCHED();                                                                                                 \
  Q_STEP(1, (C) + 1, 0, (C) + 3, 0, 2 * (C) + 1)
    Q_CHUNK(0) Q_CHUNK(1) Q_CHUNK(2) Q_CHUNK(3) Q_CHUNK(4) Q_CHUNK(5) Q_CHUNK(6) Q_CHUNK(7)
    __builtin_amdgcn_s_setprio(0);
    if (w == 0)
      asm volatile("s_mov_b32 m0, %3\n\ts_nop 0\n\tbuffer_load_dword %0, %1, %2 offen sc1 lds" ::"v"((u32)(1024 + (lane & 7) * 4)), "s"(ws), "s"(WS_HFLAG + rep), "s"(L_MISC + 768) : "memory");
    f2 cq[2], ck[2];
#pragma unroll
    for (int e = 0; e < 2; ++e) {
      cq[e] = f2{dpp<0x150>(aq[0][2 * e]), dpp<0x150>(aq[0][2 * e + 1])};
      ck[e] = f2{dpp<0x150>(ak[0][2 * e]), dpp<0x150>(ak[0][2 * e + 1])};
    }
    const float cv = asf((u32)__builtin_amdgcn_ds_bpermute(i16 * 4, (int)asu(av[0][0])));
    v2u qf[5], kf[5], vf[5];
#pragma unroll
    for (int j = 0; j < 5; ++j) {
      f2 t0 = f2{aq[j][0], aq[j][1]} - m1[j] * cq[0], t1 = f2{aq[j][2], aq[j][3]} - m1[j] * cq[1];
      qf[j] = v2u{pk(t0.x, t0.y), pk(t1.x, t1.y)};
      t0 = (f2{ak[j][0], ak[j][1]} - m1[j] * ck[0]) * r1[j]; t1 = (f2{ak[j][2], ak[j][3]} - m1[j] * ck[1]) * r1[j];
      kf[j] = v2u{pk(t0.x, t0.y), pk(t1.x, t1.y)};
    }
    SCHED();
    const f4 z4 = {0.f, 0.f, 0.f, 0.f};
    const float big = (rt == 0 && g < 2) ? -1.0e30f : 0.f;
    f4 s[5], sp[5];
#pragma unroll
    for (int j = 0; j < 5; ++j) {
      s[j] = mma16(kf[j], qf[j], j == 0 ? bc + big : bc);
      if (j > 0) sp[j] = mma16(kf[j - 1], qf[j], bp);
      const f4 t = (av[j] - m4[j] * cv) * r4[j];
      vf[j] = v2u{pk(t[0], t[1]), pk(t[2], t[3])};
      SCHED();
    }
    float mx[5];
#pragma unroll
    for (int j = 0; j < 5; ++j) {
      if (j > 0) {
#pragma unroll
        for (int e = 0; e < 4; ++e) s[j][e] = fmx(s[j][e], sp[j][e]);
      }
      mx[j] = fmx(fmx(s[j][0], s[j][1]), fmx(s[j][2], s[j][3]));
      sp[j][0] = asf((u32)__builtin_amdgcn_ds_swizzle((int)asu(mx[j]), 0x401F));
      SCHED();
    }
#pragma unroll
    for (int j = 0; j < 5; ++j) {
      mx[j] = fmx(mx[j], sp[j][0]);
      sp[j][1] = asf((u32)__builtin_amdgcn_ds_bpermute((lane ^ 32) * 4, (int)asu(mx[j])));
      r1[j] *= 0.25f * 1.44269504f;
      SCHED();
    }
    const v2u ones = {0x3F803F80u, 0x3F803F80u};
    f4 sum[5], o[5];
#pragma unroll
    for (int j = 0; j < 5; ++j) {
      const float nm = -fmx(mx[j], sp[j][1]) * r1[j];
      float p[4];
#pragma unroll
      for (int e = 0; e < 4; ++e) p[e] = __builtin_amdgcn_exp2f(__builtin_fmaf(s[j][e], r1[j], nm));
      const u32 p0 = pk(p[0], p[1]), p1 = pk(p[2], p[3]);
      sum[j] = mma16(ones, v2u{p0, p1}, z4);
      if (j == 0) o[j] = mma16(vf[0], v2u{p0, p1}, z4);
      else o[j] = mma(v4u{vf[j - 1].x, vf[j - 1].y, vf[j].x, vf[j].y}, v4u{p0 & ~mc0, p1 & ~mc1, p0 & mc0, p1 & mc1}, z4);
      SCHED();
    }
    v2u op[5];
#pragma unroll
    for (int j = 0; j < 5; ++j) {
      const f4 y = o[j] * __builtin_amdgcn_rcpf(sum[j][0]);
      op[j] = v2u{pk(y[0], y[1]), pk(y[2], y[3])};
    }
#pragma unroll
    for (int P = 0; P < 3; ++P) {
      const v2u a = op[2 * P], c = op[P < 2 ? 2 * P + 1 : 4];
      const auto r0 = __builtin_amdgcn_permlane16_swap(a.x, c.x, false, false), r1 = __builtin_amdgcn_permlane16_swap(a.y, c.y, false, false);
      st128(v4u{(u32)r0[0], (u32)r1[0], (u32)r0[1], (u32)r1[1]}, ws, vo[P]);
    }
    if (w < 4) { WAITV(3); lpost0(C_WO); }
    WAITV(0);
    if (arrive(C_P) == 7) post(ws, WS_OFLAG + (u32)((rt * 8 + hg) * 32), lane, tl, th);
  } else {
    const u32 vo = WS_U + (u32)(((xcd >> 1) * 8 + w) * 43 * 1024 + lane * 16);
#pragma unroll 1
    for (int n = 0; n < 43; ++n) st128(v4u{SENT, SENT, SENT, SENT}, ws, vo + (u32)(n * 1024));
    WAITV(0);
    BAR();
    make_tag();
    if (w == 0) post(ws, WS_HFLAG + 1024 + (u32)((xcd >> 1) * 8), lane, tl, th);
  }

  const int dh = w & 1, kh = (w >> 1) & 1;
  const u32 vl = (u32)(lane * 16);
  constexpr u32 L_RED = 81920;
  u32 wa[4];
#pragma unroll
  for (int c = 0; c < 4; ++c) wa[c] = wbase ^ (u32)(c << 5);
  if (w >= 4) {
    const int orow = (tid & 255) >> 3;
    const u32 ov = (u32)((64 * st + orow) * 1024 + T * 128 + (tid & 7) * 16);
    v4u xr[2];
    u32 b2r[2];
    {
      const rsrc_t rb2 = mk(pb2, S * 2);
      asm volatile("buffer_load_dwordx4 %0, %4, %6, 0 offen\n\tbuffer_load_dwordx4 %1, %4, %6, %8 offen\n\t"
                   "buffer_load_ushort %2, %5, %7, 0 offen\n\tbuffer_load_ushort %3, %5, %7, 0 offen offset:64"
                   : "=&v"(xr[0]), "=&v"(xr[1]), "=&v"(b2r[0]), "=&v"(b2r[1])
                   : "v"(ov), "v"((u32)((64 * st + orow) * 2)), "s"(rx), "s"(rb2), "s"(32768u));
    }
    if (role == 0) {
      if (w == 4) {
        lwait<1>(C_G, 4);
        poll1(ws, (u32)(1024 + (lane & 3) * 8), WS_HFLAG + rep, tl, th);
        lpost(C_OK);
      }
      lwait<1>(C_G + 8, 4);
#pragma unroll
      for (int n = 0; n < 16; ++n) { WAITV(PACE3); wo_issue((u32)(n * 4096)); }
#pragma unroll
      for (int n = 0; n < 8; ++n) { WAITV(PACE3); if (n == PACE3) lpost(C_WO); w2a_issue((u32)(n * 4096)); }
      lwait(C_WO, 4);
    } else if (role == 2) {
#pragma unroll 1
      for (u32 t = 0; t < 32768; t += 4096) { glds<0, 0>(rwo, v_wl, t << 3, ql + t); glds<32768, 0>(rwo, v_wh, t << 3, ql + t); WAITV(PACE2); }
#pragma unroll 1
      for (u32 t = 0; t < 49152; t += 4096) { glds<65536, 0>(rw2, v_w2a, t << 5, ql + t); WAITV(PACE2); }
#pragma unroll
      for (int n = 12; n < 17; ++n) w2a_issue((u32)(n * 4096));
      WAITV(0);
      hbar(C_WO);
    } else {
      lwait<1>(C_Q7, 8);
#pragma unroll
      for (int n = 11; n < 16; ++n) wo_issue((u32)(n * 4096));
#pragma unroll
      for (int n = 0; n < 6; ++n) w2a_issue((u32)(n * 4096));
      WAITV(6); lpost0(C_WO); lwait(C_WO, 8);
    }
    asm volatile("" : "+v"(xr[0]), "+v"(xr[1]), "+v"(b2r[0]), "+v"(b2r[1]));
    v4u wr[8][4];
#pragma unroll
    for (int d = 0; d < 4; ++d) { wa[d] += (u32)(kh * 32768); asm volatile("" : "+v"(wa[d])); }
#pragma unroll
    for (int c = 0; c < 8; ++c) {
      if (c < 6) w2a_issue((u32)((17 + c) * 4096));
      if (c == 5) { WAITV(6); lpost0(C_A); }
#pragma unroll
      for (int d = 0; d < 4; ++d) wr[c][d] = wfrag(wa[d] + (u32)(c * 4096));
    }
    lpost0(C_WR);
    if (role == 0) {
      lwait<1>(C_G + 12, 4);
#pragma unroll
      for (int n = 8; n < 17; ++n) { WAITV(PACE2); w2a_issue((u32)(n * 4096)); }
    }
    const u32 vf = (u32)((((4 * st + ((lane >> 3) & 1) * 3) * 57) >> 9) * 256 + (lane & 7) * 32);
    v2u fo;
    lwait(C_WR, 4);
    const bool ofirst = role == 0;
    if (!ofirst) {
#pragma unroll
      for (int n = NB1; n < 16; ++n) { WAITV(PACE2); w2b_issue((u32)(n * 4096)); }
    }
#pragma unroll
    for (int n = 0; n < NB1 - 6; ++n) { WAITV(PACE2); w2b_issue((u32)(n * 4096)); }
    asm volatile("buffer_load_dwordx2 %0, %1, %2, %3 offen sc1" : "=v"(fo) : "v"(vf), "s"(ws), "s"(WS_OFLAG + rep));
#pragma unroll
    for (int n = NB1 - 6; n < NB1; ++n) w2b_issue((u32)(n * 4096));
    asm volatile("s_waitcnt vmcnt(6)" : "+v"(fo)::"memory");
    const bool fast = __all(fo.x == tl && fo.y == th);
    auto b_rest = [&]() __attribute__((always_inline)) {
      if (ofirst) {
#pragma unroll
        for (int n = NB1; n < 16; ++n) { WAITV(PACE2); w2b_issue((u32)(n * 4096)); }
      }
      lwait<1>(C_PRE, 4);
#pragma unroll
      for (int n = 16; n < 20; ++n) w2b_issue((u32)(n * 4096));
      WAITV(0);
      lpost(C_B0);
    };
    if (!fast) {
      b_rest();
      lpost0(C_YB);
      if (w == 4) poll1(ws, vf, WS_OFLAG + rep, tl, th);
      else lwait<1>(C_O, 1);
    }
    if (w == 4) lpost(C_O);
    FENCE();
    f4 ao[2][4];
    {
      v4u of[8][2];
      const u32 so = WS_O + (u32)((4 * st + 2 * dh) * 16384 + (kh ^ vw) * 8192);
#pragma unroll
      for (int c = 0; c < 16; ++c) {
        of[c >> 1][c & 1] = ld128(ws, vl + ((c >> 1) & 3) * 1024, so + (u32)((c & 1) * 16384 + (c >> 3) * 4096));
        if (c == 15 && fast) lpost0(C_YB);
        SCHED();
      }
#pragma unroll
      for (int c = 0; c < 8; ++c)
#pragma unroll
        for (int a = 0; a < 2; ++a) {
#pragma unroll
          for (int d = 0; d < 4; ++d) ao[a][d] = mma(wr[c][d], of[c][a], c == 0 ? f4{0.f, 0.f, 0.f, 0.f} : ao[a][d]);
          SCHED();
        }
    }
    f4 y[2][2];
#pragma unroll
    for (int p = 0; p < 2; ++p) {
      const float b2 = bflo(b2r[p]);
      y[p][0] = f4{bflo(xr[p][0]), bfhi(xr[p][0]), bflo(xr[p][1]), bfhi(xr[p][1])} + b2;
      y[p][1] = f4{bflo(xr[p][2]), bfhi(xr[p][2]), bflo(xr[p][3]), bfhi(xr[p][3])} + b2;
    }
    u32 rb = L_RED + (u32)(kh * 17408 + (dh * 32 + 2 * (i16 & 7) + (i16 >> 3)) * 272 + g * 16);
    u32 ra = L_RED + (u32)(orow * 272 + (tid & 7) * 32);
    asm volatile("" : "+v"(y[0][0]), "+v"(y[0][1]), "+v"(y[1][0]), "+v"(y[1][1]), "+v"(ra), "+v"(rb));
    if (fast) b_rest();
    lwait<1>(C_D1, 4);
#pragma unroll
    for (int a = 0; a < 2; ++a)
#pragma unroll
      for (int d = 0; d < 4; ++d) *(LAS f4*)(rb + a * 16 * 272 + d * 64) = ao[a][d];
    hbar(C_X);
#pragma unroll
    for (int p = 0; p < 2; ++p) {
#pragma unroll
      for (int h = 0; h < 2; ++h) y[p][h] += *(LAS f4*)(ra + p * 32 * 272 + h * 16) + *(LAS f4*)(ra + 17408 + p * 32 * 272 + h * 16);
    }
    asm volatile("" : "+v"(y[0][0]), "+v"(y[0][1]), "+v"(y[1][0]), "+v"(y[1][1]));
    if (b == 249 && w == 4) {
#pragma unroll 1
      for (u32 n = 0; n < SPIN_CAP; ++n) {
        const v4u f0 = ld128(ws, vl, WS_HFLAG + rep);
        const v2u f1 = ld64(ws, (u32)(1024 + (lane & 7) * 8), WS_HFLAG + rep), f2 = ld64(ws, (u32)(lane * 32), WS_OFLAG + rep),
                  f3 = ld64(ws, (u32)((lane < 56 ? lane + 64 : 0) * 32), WS_OFLAG + rep);
        const u32 d = ((f0.x ^ tl) | (f0.z ^ tl) | (f1.x ^ tl)) | ((f2.x ^ tl) | (f3.x ^ tl)) | ((f0.y ^ th) | (f0.w ^ th) | (f1.y ^ th)) | ((f2.y ^ th) | (f3.y ^ th));
        if (__all(d == 0)) break;
        NAP();
      }
      FENCE();
      if (lane == 0) st64(v2u{tl, th}, ws, WS_E);
    }
    __builtin_amdgcn_s_setprio(1);
    BAR0();
#pragma unroll
    for (int p = 0; p < 2; ++p) {
#pragma unroll
      for (int h = 0; h < 2; ++h) y[p][h] += *(LAS f4*)(ra + 2 * 17408 + p * 32 * 272 + h * 16) + *(LAS f4*)(ra + 3 * 17408 + p * 32 * 272 + h * 16);
      __builtin_amdgcn_raw_buffer_store_b128(v4u{pk(y[p][0][0], y[p][0][1]), pk(y[p][0][2], y[p][0][3]), pk(y[p][1][0], y[p][1][1]), pk(y[p][1][2], y[p][1][3])}, mk(pout, S * D * 2), ov + p * 32768, 0, 0);
    }
  } else {
    const u32 u0 = WS_U + (u32)(((4 * T + 2 * dh) * 43 + kh) * 1024);
    const u32 ua = u0 + (u32)(fa0 * 1024), uh = u0 + (u32)(fa1 * 1024), ub = u0 + (u32)(fb0 * 1024);
    const u32 vl2 = vl + 43 * 1024, u5b = kh ? OOBS : uh + 8192;
#pragma unroll
    for (int c = 0; c < 4; ++c) { wa[c] += (u32)(65536 + kh * 4096); asm volatile("" : "+v"(wa[c])); }
    v4u pre[2][4], uf[UW][4];
    f4 acc[4][2];
    auto u_load = [&](int G, int k) __attribute__((always_inline)) {
      return ld128(ws, ((k & 1) ? vl2 : vl) + (k >> 1) * 2048,
                   G < 3 ? ua + (u32)(G * 4096) : (G == 5 && k >= 2) ? u5b : G < 6 ? uh + (u32)((G - 3) * 4096) : ub + (u32)((G - 6) * 4096));
    };
    u32 gate = 0;
    if (role != 0 && w == 0) {
      const v2u fs = *(LAS v2u*)(L_MISC + 768 + (lane & 3) * 8);
      if (role == 2 || !__all(fs.x == tl && fs.y == th)) poll1(ws, (u32)(1024 + (lane & 3) * 8), WS_HFLAG + rep, tl, th);
      FENCE();
      gate = __builtin_amdgcn_raw_buffer_load_b32(ws, (u32)((lane & 15) * 8), ua, SC1);
    }
    lwait<1>(C_A, 4);
#pragma unroll
    for (int n = 0; n < 2; ++n)
#pragma unroll
      for (int c = 0; c < 4; ++c) { pre[n][c] = wfrag(wa[c] + (u32)(n * 8192)); acc[c][n] = f4{0.f, 0.f, 0.f, 0.f}; }
    lpost0(C_PRE);
    if (role != 0 && w == 0) {
#pragma unroll 1
      for (u32 n = 0; n < SPIN_CAP && stale(gate); ++n) {
        asm volatile("s_sleep 2" ::: "memory");
        gate = __builtin_amdgcn_raw_buffer_load_b32(ws, (u32)((lane & 15) * 8), ua, SC1);
      }
      lpost(C_OK);
    }
    lwait(C_OK, 1);
    lwait<1>(C_YB, 4);
#pragma unroll
    for (int n = 0; n < 4 * URAMP; ++n) { uf[n >> 2][n & 3] = u_load(n >> 2, n & 3); SCHED(); }
    u32 seen = 0, rb = L_RED + (u32)((2 + kh) * 17408 + i16 * 272 + dh * 128 + g * 16);
    asm volatile("" : "+v"(rb));
    const u32 skip = (u32)(kh * 8192), back = skip - 131072u;
    u64 bad = __builtin_amdgcn_ballot_w64(smax(uf[0][3], smax(uf[0][2], smax(uf[0][1], smax(uf[0][0], 0)))) == SENT);
#pragma unroll
    for (int G = 0; G < 11; ++G) {
      v4u(&u)[4] = uf[G % UW];
      v4u(&v)[4] = uf[(G + 1) % UW];
      if (__builtin_expect(bad != 0, 0)) {
#pragma unroll 1
        for (u32 n = 0; n < SPIN_CAP; ++n) {
#pragma unroll
          for (int k = 0; k < 4; ++k) u[k] = u_load(G, k);
          if (!stale(smax(u[3], smax(u[2], smax(u[1], smax(u[0], 0)))))) break;
          asm volatile("s_sleep 4" ::: "memory");
        }
      }
      if (G == 5) {
        lpost0(C_DONE);
        if (__builtin_expect(__builtin_amdgcn_readfirstlane(seen) < 4, 0)) lwait(C_B0, 4);
      }
      if (G == 3) lpost0(C_D1);
      u32 ma = 0;
#pragma unroll
      for (int m = 0; m < 16; ++m) {
        const int k = m >> 3, ci = (m >> 1) & 3, c = (G & 1) ? 3 - ci : ci, t = m & 1, n = 2 * G + k, nn = n + 2 < 12 ? n + 2 : n - 10;
        acc[c][t] = mma(u[2 * k + t], pre[k][c], acc[c][t]);
        if ((G == 3 || G == 5 || G == 9) && k == 0 && t == 0) { wa[c] += G == 5 ? back : 65536u; asm volatile("" : "+v"(wa[c])); }
        if (G == 4 && k == 1 && t == 0) { wa[c] -= skip; asm volatile("" : "+v"(wa[c])); }
        if (t == 1 && n < 20) whalf(pre[k][c], 0, wa[c] + (u32)((nn & 7) * 8192));
        if (t == 0 && m > 0 && n - (ci == 0) < 20) {
          const int pi = (ci + 3) & 3, pc = (G & 1) ? 3 - pi : pi, pk = ci == 0 ? 0 : k, pn = ci == 0 ? nn - 1 : nn;
          whalf(pre[pk][pc], 1, wa[pc] + (u32)((pn & 7) * 8192));
        }
        if (m == 15 && n < 20) whalf(pre[1][c], 1, wa[c] + (u32)((nn & 7) * 8192));
        if (G == 0 && m < 8 && 4 * URAMP + m < 4 * UW) uf[(4 * URAMP + m) >> 2][m & 3] = u_load((4 * URAMP + m) >> 2, m & 3);
        if ((m == 9 || m == 13) && G + UW < 11) u[(m - 9) >> 2] = u_load(G + UW, (m - 9) >> 2);
        if ((m == 0 || m == 3) && G > 0 && G + UW - 1 < 11) uf[(G + UW - 1) % UW][2 + (m != 0)] = u_load(G + UW - 1, 2 + (m != 0));
        if (G < 10 && (m == 5 || m == 7 || m == 10 || m == 11)) ma = smax(v[m == 5 ? 3 : m == 7 ? 2 : 11 - m], ma);
        if (G < 10 && m == 12) bad = __builtin_amdgcn_ballot_w64(ma == SENT);
        if (n == 8 && m == 6) seen = lpeek(C_B0);
        if (n == 20 && m == 0) seen = lpeek(C_DONE);
        SCHED();
      }
    }
    if (__builtin_expect(__builtin_amdgcn_readfirstlane(seen) < 4, 0)) lwait(C_DONE, 4);
#pragma unroll
    for (int c = 0; c < 4; ++c) {
      *(LAS f4*)(rb + c * 16 * 272) = acc[c][0];
      *(LAS f4*)(rb + c * 16 * 272 + 64) = acc[c][1];
    }
    BAR();
  }
  asm volatile(".Lfk_end:" ::: "memory");
}

extern "C" void kernel_launch(void* const* d_in, const int* in_sizes, int n_in, void* d_out, int out_size, void* d_ws, size_t ws_size,
                              hipStream_t stream) {
  fused_kernel<<<256, 512, 0, stream>>>(d_in[0], d_in[1], d_in[2], d_in[4], d_in[5], d_in[6], d_in[7], d_out, d_ws);
}
```
